```python
import torch

def _softmax(x, axis=-1):
    return torch.softmax(x, dim=axis)
def _gelu(x):
    return torch.nn.functional.gelu(x, approximate="tanh")
def _silu(x):
    return torch.nn.functional.silu(x)
def _sigmoid(x):
    return torch.sigmoid(x)
def _layernorm(x, axis=-1, eps=1e-5):
    m = x.mean(dim=axis, keepdim=True)
    v = x.var(dim=axis, keepdim=True, unbiased=False)
    return (x - m) / torch.sqrt(v + eps)
def _scatter_add(tgt, idx, src, axis=0):
    return tgt.clone().index_add_(axis, idx, src)
def _segment_sum(src, seg, n):
    out = torch.zeros((n,) + src.shape[1:], dtype=src.dtype)
    return out.index_add_(0, seg, src)
def _pad(x, pw):
    flat = tuple(v for pair in reversed(pw) for v in pair)
    return torch.nn.functional.pad(x, flat)

kernel_name = 'concat_heads__mha_block_cached__mha_block_cached__near_ridge'

def setup_inputs():

    _w = (lambda a: (lambda t: t.to(torch.bfloat16) if t.is_floating_point() else t)(torch.as_tensor(a)))
    def _A_setup():
        B, Sq, D = 32, 64, 512
        H, Sk, Dh = 32, 512, 128
        return {
            "x": _w((lambda *s: torch.randn(*s, dtype=torch.bfloat16) * 0.8)(B, Sq, D)),
            "k_cache": _w((lambda *s: torch.randn(*s, dtype=torch.bfloat16) * 0.8)(B, H, Sk, Dh)),
            "v_cache": _w((lambda *s: torch.randn(*s, dtype=torch.bfloat16) * 0.05)(B, H, Sk, Dh)),
            "wq": _w((lambda *s: torch.randn(*s, dtype=torch.bfloat16) * 0.8)(D, H * Dh) * (D ** -0.5)),
            "wo": _w((lambda *s: torch.randn(*s, dtype=torch.bfloat16) * 0.05)(H * Dh, D) * ((H * Dh) ** -0.5)),
        }
    _A_in = _A_setup()

    def _B_setup():
        B, Sq, D = 32, 64, 512
        H, Sk, Dh = 32, 512, 128
        return {
            "x": _w((lambda *s: torch.randn(*s, dtype=torch.bfloat16) * 0.8)(B, Sq, D)),
            "k_cache": _w((lambda *s: torch.randn(*s, dtype=torch.bfloat16) * 0.8)(B, H, Sk, Dh)),
            "v_cache": _w((lambda *s: torch.randn(*s, dtype=torch.bfloat16) * 0.05)(B, H, Sk, Dh)),
            "wq": _w((lambda *s: torch.randn(*s, dtype=torch.bfloat16) * 0.8)(D, H * Dh) * (D ** -0.5)),
            "wo": _w((lambda *s: torch.randn(*s, dtype=torch.bfloat16) * 0.05)(H * Dh, D) * ((H * Dh) ** -0.5)),
        }
    _B_in = _B_setup()

    return {
        "A_x": _A_in["x"],
        "A_k_cache": _A_in["k_cache"],
        "A_v_cache": _A_in["v_cache"],
        "A_wq": _A_in["wq"],
        "A_wo": _A_in["wo"],
        "B_x": _B_in["x"],
        "B_k_cache": _B_in["k_cache"],
        "B_v_cache": _B_in["v_cache"],
        "B_wq": _B_in["wq"],
        "B_wo": _B_in["wo"],
    }

def reference(A_x, A_k_cache, A_v_cache, A_wq, A_wo, B_x, B_k_cache, B_v_cache, B_wq, B_wo):
    def _A_ref(x, k_cache, v_cache, wq, wo):
        B, Sq, D = x.shape
        H, Dh = 32, 128
        q = (lambda x, *d: x.permute(*d))((x @ wq).reshape(B, Sq, H, Dh), 0, 2, 1, 3)
        scores = torch.einsum("bhqd,bhkd->bhqk", q, k_cache) * (Dh ** -0.5)
        p = _softmax(scores, axis=-1)
        o = torch.einsum("bhqk,bhkd->bhqd", p, v_cache)
        return (lambda x, *d: x.permute(*d))(o, 0, 2, 1, 3).reshape(B, Sq, H * Dh) @ wo

    def _B_ref(x, k_cache, v_cache, wq, wo):
        B, Sq, D = x.shape
        H, Dh = 32, 128
        q = (lambda x, *d: x.permute(*d))((x @ wq).reshape(B, Sq, H, Dh), 0, 2, 1, 3)
        scores = torch.einsum("bhqd,bhkd->bhqk", q, k_cache) * (Dh ** -0.5)
        p = _softmax(scores, axis=-1)
        o = torch.einsum("bhqk,bhkd->bhqd", p, v_cache)
        return (lambda x, *d: x.permute(*d))(o, 0, 2, 1, 3).reshape(B, Sq, H * Dh) @ wo

    a = _A_ref(A_x, A_k_cache, A_v_cache, A_wq, A_wo)
    b = _B_ref(B_x, B_k_cache, B_v_cache, B_wq, B_wo)
    return torch.concatenate([a, b], axis=-1)
```

```cpp
#include <hip/hip_runtime.h>
#include <hip/hip_bf16.h>
#include <stdint.h>

typedef short bf16x8 __attribute__((ext_vector_type(8)));
typedef short s16x4 __attribute__((ext_vector_type(4)));
typedef float f32x4 __attribute__((ext_vector_type(4)));
typedef float f32x2 __attribute__((ext_vector_type(2)));
typedef unsigned u32x4 __attribute__((ext_vector_type(4)));
typedef unsigned u32x2 __attribute__((ext_vector_type(2)));
typedef __bf16 bf16x2_t __attribute__((ext_vector_type(2)));
#define LAS __attribute__((address_space(3)))
#define DI __device__ __forceinline__

constexpr int NB = 32, SQ = 64, DM = 512, NH = 32, SK = 512, DH = 128, HPG = 8;
#define NKV 5
constexpr int NW = 4;
constexpr int W2_OFF = 0, W3_OFF = W2_OFF + NW * 8192, KV_OFF = W3_OFF + NW * 8192, OB_OFF = KV_OFF + NKV * 16384;
constexpr int LDS_BYTES = OB_OFF + 16384;
static_assert(LDS_BYTES <= 163840, "LDS");
constexpr float QSCALE = 0.08838834764831845f * 1.4426950408889634f;

#define Z4 (f32x4{0.f, 0.f, 0.f, 0.f})
#define MFMA(a, b, c) __builtin_amdgcn_mfma_f32_16x16x32_bf16((a), (b), (c), 0, 0, 0)

DI unsigned pk2(float a, float b) { f32x2 v = {a, b}; return __builtin_bit_cast(unsigned, __builtin_convertvector(v, bf16x2_t)); }
DI bf16x8 pack8(const f32x4& a, const f32x4& b) {
  u32x4 w = {pk2(a[0], a[1]), pk2(a[2], a[3]), pk2(b[0], b[1]), pk2(b[2], b[3])};
  return __builtin_bit_cast(bf16x8, w);
}
DI bf16x8 lds_b128(unsigned addr) { return *(const LAS bf16x8*)(uintptr_t)addr; }
DI s16x4 lds_tr(unsigned addr) { return __builtin_amdgcn_ds_read_tr16_b64_v4i16((LAS s16x4*)(uintptr_t)addr); }
template <int USTEP> DI bf16x8 tr_frag(unsigned a) {
  s16x4 lo = lds_tr(a), hi = lds_tr(a + USTEP);
  return __builtin_shufflevector(lo, hi, 0, 1, 2, 3, 4, 5, 6, 7);
}
DI float xmax16_32(float v) {
  auto r = __builtin_amdgcn_permlane16_swap(__float_as_uint(v), __float_as_uint(v), false, false);
  v = fmaxf(__uint_as_float(r[0]), __uint_as_float(r[1]));
  auto q = __builtin_amdgcn_permlane32_swap(__float_as_uint(v), __float_as_uint(v), false, false);
  return fmaxf(__uint_as_float(q[0]), __uint_as_float(q[1]));
}
DI float xsum16_32(float v) {
  auto r = __builtin_amdgcn_permlane16_swap(__float_as_uint(v), __float_as_uint(v), false, false);
  v = __uint_as_float(r[0]) + __uint_as_float(r[1]);
  auto q = __builtin_amdgcn_permlane32_swap(__float_as_uint(v), __float_as_uint(v), false, false);
  return __uint_as_float(q[0]) + __uint_as_float(q[1]);
}
#define BAR() asm volatile("s_waitcnt lgkmcnt(0)\n\ts_barrier" ::: "memory")

#define DMA8(NTS)                                                                                   \
  asm volatile("s_mov_b32 %0, m0\n\ts_mov_b32 m0, %7\n\ts_mov_b32 %1, %8\n\t"                       \
               "buffer_load_dwordx4 %2, %6, %1 offen " NTS "lds\n\t"                                \
               "s_add_u32 m0, m0, 0x400\n\ts_add_u32 %1, %1, %9\n\t"                                \
               "buffer_load_dwordx4 %3, %6, %1 offen " NTS "lds\n\t"                                \
               "s_add_u32 m0, m0, 0x400\n\ts_add_u32 %1, %1, %9\n\t"                                \
               "buffer_load_dwordx4 %4, %6, %1 offen " NTS "lds\n\t"                                \
               "s_add_u32 m0, m0, 0x400\n\ts_add_u32 %1, %1, %9\n\t"                                \
               "buffer_load_dwordx4 %5, %6, %1 offen " NTS "lds\n\t"                                \
               "s_add_u32 m0, m0, 0x400\n\ts_add_u32 %1, %1, %9\n\t"                                \
               "buffer_load_dwordx4 %2, %6, %1 offen " NTS "lds\n\t"                                \
               "s_add_u32 m0, m0, 0x400\n\ts_add_u32 %1, %1, %9\n\t"                                \
               "buffer_load_dwordx4 %3, %6, %1 offen " NTS "lds\n\t"                                \
               "s_add_u32 m0, m0, 0x400\n\ts_add_u32 %1, %1, %9\n\t"                                \
               "buffer_load_dwordx4 %4, %6, %1 offen " NTS "lds\n\t"                                \
               "s_add_u32 m0, m0, 0x400\n\ts_add_u32 %1, %1, %9\n\t"                                \
               "buffer_load_dwordx4 %5, %6, %1 offen " NTS "lds\n\t"                                \
               "s_mov_b32 m0, %0"                                                                   \
               : "=&s"(keep), "=&s"(tmp)                                                            \
               : "v"(v0), "v"(v1), "v"(v2), "v"(v3), "s"(srd), "s"(lds), "s"(so), "s"(ps)           \
               : "memory", "scc")
template <bool NT>
DI void dma8(__amdgpu_buffer_rsrc_t srd, unsigned v0, unsigned v1, unsigned v2, unsigned v3, unsigned lds, unsigned so, unsigned ps) {
  unsigned keep, tmp;
  if (NT) DMA8("nt "); else DMA8("");
}
DI void wait_vm(int newer) {
  if (newer >= 5) asm volatile("s_waitcnt vmcnt(40)" ::: "memory");
  else if (newer == 4) asm volatile("s_waitcnt vmcnt(32)" ::: "memory");
  else if (newer == 3) asm volatile("s_waitcnt vmcnt(24)" ::: "memory");
  else if (newer == 2) asm volatile("s_waitcnt vmcnt(16)" ::: "memory");
  else if (newer == 1) asm volatile("s_waitcnt vmcnt(8)" ::: "memory");
  else asm volatile("s_waitcnt vmcnt(0)" ::: "memory");
}

struct Stream {
  __amdgpu_buffer_rsrc_t sA, sB;
  unsigned a0, a1, a2, a3, b0, b1;
  int w;
  int ki, kc;
  unsigned islot;
  int hbase;
};
DI void stream_issue(Stream& st) {
  const int k = st.ki;
  if (st.w < 2) {
    const unsigned so = (unsigned)(k >> 1) * 16384u + (unsigned)st.w * 8192u;
    const unsigned lds = KV_OFF + st.islot + (unsigned)st.w * 8192u;
    if (k & 1) dma8<true>(st.sB, st.b0, st.b1, st.b0, st.b1, lds, so, 1024u);
    else dma8<true>(st.sA, st.a0, st.a1, st.a2, st.a3, lds, so, 1024u);
    st.islot += 16384u; if (st.islot == NKV * 16384u) st.islot = 0;
  } else {
    const int odd = st.w & 1;
    const unsigned lds = (odd ? W3_OFF : W2_OFF) + st.islot;
    int h, t; bool isq;
    if (k < 8) { isq = true; h = 0; t = 2 * k + odd; }
    else if (k >= 120) { isq = false; h = 7; t = 2 * (k - 120) + odd; }
    else { isq = !odd; h = ((k - 8) >> 4) + (odd ? 0 : 1); t = (k - 8) & 15; }
    if (isq) {
      const unsigned so = (unsigned)(32 * t) * 8192u + (unsigned)(st.hbase + h) * 256u;
      dma8<false>(st.sA, st.a0, st.a0, st.a1, st.a1, lds, so, 4u * 8192u);
    } else {
      const unsigned so = (unsigned)((st.hbase + h) * 128 + 32 * (t >> 2)) * 1024u + (unsigned)(t & 3) * 256u;
      dma8<false>(st.sB, st.b0, st.b1, st.b0, st.b1, lds, so, 4u * 1024u);
    }
    st.islot += 8192u; if (st.islot == NW * 8192u) st.islot = 0;
  }
  st.ki = k + 1;
}

__global__ __launch_bounds__(512, 2) void fused_kernel(
    const __hip_bfloat16* __restrict__ xA, const __hip_bfloat16* __restrict__ kA, const __hip_bfloat16* __restrict__ vA,
    const __hip_bfloat16* __restrict__ wqA, const __hip_bfloat16* __restrict__ woA,
    const __hip_bfloat16* __restrict__ xB, const __hip_bfloat16* __restrict__ kB, const __hip_bfloat16* __restrict__ vB,
    const __hip_bfloat16* __restrict__ wqB, const __hip_bfloat16* __restrict__ woB, float* __restrict__ part) {
  __shared__ __attribute__((aligned(1024))) char smem[LDS_BYTES];
  const unsigned L0 = (unsigned)(uintptr_t)(LAS char*)smem;
  const int tid = threadIdx.x, lane = tid & 63;
  const int wave = __builtin_amdgcn_readfirstlane(tid >> 6);
  const int grp = blockIdx.x & 7, b = blockIdx.x >> 3, br = grp >> 2, hg = grp & 3;
  const int i16 = lane & 15, g = lane >> 4, qq = i16 >> 2, pp = i16 & 3;
  const unsigned rho = 4 * (g & 1) + qq;
  const unsigned trP = L0 + 256u * (4 * g + qq) + 32u * rho + 8u * pp;
  const unsigned trN = L0 + 256u * (8 * g + qq) + 32u * rho + 8u * pp;

  if (wave < 4) {
    const __hip_bfloat16* x = br ? xB : xA;
    bf16x8 xf[16];
    {
      const char* xr = (const char*)(x + ((size_t)b * SQ + 16 * wave + i16) * DM) + 16 * g;
#pragma unroll
      for (int s = 0; s < 16; ++s) xf[s] = *(const bf16x8*)(xr + 64 * s);
    }
    unsigned kofs[4];
#pragma unroll
    for (int s = 0; s < 4; ++s) kofs[s] = L0 + KV_OFF + 256u * i16 + 16u * ((4 * s + g) ^ i16);
    f32x4 qacc[8];
    bf16x8 qf[4];
#define QPROJ(S, TOFF, FIRST)                                                                     \
    _Pragma("unroll") for (int c = 0; c < 8; ++c) {                                               \
      const bf16x8 a = tr_frag<1024>((trN ^ (32u * c)) + (TOFF));                                 \
      qacc[c] = MFMA(a, xf[S], (FIRST) ? Z4 : qacc[c]);                  \
    }
#define QFIN()                                                                                    \
    _Pragma("unroll") for (int s = 0; s < 4; ++s) {                                               \
      const f32x4 a = qacc[2 * s] * QSCALE, c = qacc[2 * s + 1] * QSCALE;                         \
      unsigned a0 = pk2(a[0], a[1]), a1 = pk2(a[2], a[3]), b0 = pk2(c[0], c[1]), b1 = pk2(c[2], c[3]); \
      auto r0 = __builtin_amdgcn_permlane32_swap(a0, b0, false, false);                           \
      auto r1 = __builtin_amdgcn_permlane32_swap(a1, b1, false, false);                           \
      auto t0 = __builtin_amdgcn_permlane16_swap(r0[0], r0[1], false, false);                     \
      auto t1 = __builtin_amdgcn_permlane16_swap(r1[0], r1[1], false, false);                     \
      u32x4 w = {t0[0], t1[0], t0[1], t1[1]};                                                     \
      qf[s] = __builtin_bit_cast(bf16x8, w);                                                      \
    }
#pragma unroll
    for (int e = 0; e < 8; ++e) {
      BAR();
      QPROJ(2 * e, W2_OFF + (e & 3) * 8192, e == 0);
      QPROJ(2 * e + 1, W3_OFF + (e & 3) * 8192, false);
    }
    QFIN();
    unsigned kvs = 0;
    for (int h = 0; h < HPG; ++h) {
      f32x4 o[8];
      float m = 0.f, lsum = 0.f;
#pragma unroll
      for (int i = 0; i < 8; ++i) {
        BAR();
        f32x4 sc[4];
#pragma unroll
        for (int t = 0; t < 4; ++t) {
#pragma unroll
          for (int s = 0; s < 4; ++s) {
            const bf16x8 a = lds_b128(kofs[s] + kvs + 4096u * t);
            sc[t] = MFMA(a, qf[s], s == 0 ? Z4 : sc[t]);
          }
        }
        QPROJ(2 * i, W2_OFF + ((2 * i) & 3) * 8192, i == 0);
        float mx = fmaxf(fmaxf(sc[0][0], sc[0][1]), fmaxf(sc[0][2], sc[0][3]));
#pragma unroll
        for (int t = 1; t < 4; ++t) mx = fmaxf(fmaxf(mx, fmaxf(sc[t][0], sc[t][1])), fmaxf(sc[t][2], sc[t][3]));
        mx = xmax16_32(mx);
        float alpha = 1.f;
        if (i == 0) m = mx;
        else { const float mn = fmaxf(m, mx); alpha = __builtin_amdgcn_exp2f(m - mn); m = mn; }
        float ps = 0.f;
#pragma unroll
        for (int t = 0; t < 4; ++t)
#pragma unroll
          for (int r = 0; r < 4; ++r) { sc[t][r] = __builtin_amdgcn_exp2f(sc[t][r] - m); ps += sc[t][r]; }
        lsum = (i == 0) ? ps : lsum * alpha + ps;
        const bf16x8 p0 = pack8(sc[0], sc[1]), p1 = pack8(sc[2], sc[3]);
        kvs += 16384u; if (kvs == NKV * 16384u) kvs = 0;
        BAR();
#pragma unroll
        for (int c = 0; c < 8; ++c) {
          const unsigned va = (trP ^ (32u * c)) + KV_OFF + kvs;
          const bf16x8 a0 = tr_frag<4096>(va), a1 = tr_frag<4096>(va + 8192u);
          o[c] = MFMA(a0, p0, i == 0 ? Z4 : o[c] * alpha);
          o[c] = MFMA(a1, p1, o[c]);
        }
        QPROJ(2 * i + 1, W2_OFF + ((2 * i + 1) & 3) * 8192, false);
        kvs += 16384u; if (kvs == NKV * 16384u) kvs = 0;
      }
      const float inv = 1.f / xsum16_32(lsum);
#pragma unroll
      for (int s = 0; s < 4; ++s)
        *(LAS bf16x8*)(uintptr_t)(L0 + OB_OFF + (wave * 4 + s) * 1024 + lane * 16) = pack8(o[2 * s] * inv, o[2 * s + 1] * inv);
      QFIN();
    }
#pragma unroll
    for (int e = 0; e < 8; ++e) BAR();
  } else {
    const int w = wave - 4;
    Stream st;
    st.w = w; st.ki = 0; st.kc = 0; st.islot = 0; st.hbase = hg * HPG;
    const unsigned r4 = lane >> 4, c16 = lane & 15;
    if (w < 2) {
      const size_t off = ((size_t)b * NH + hg * HPG) * SK * DH;
      st.sA = __builtin_amdgcn_make_buffer_rsrc((void*)((br ? kB : kA) + off), (short)0, HPG * SK * DH * 2, 0x00020000);
      st.sB = __builtin_amdgcn_make_buffer_rsrc((void*)((br ? vB : vA) + off), (short)0, HPG * SK * DH * 2, 0x00020000);
      st.a0 = r4 * 256u + 16u * (c16 ^ (r4));      st.a1 = r4 * 256u + 16u * (c16 ^ (4 + r4));
      st.a2 = r4 * 256u + 16u * (c16 ^ (8 + r4));  st.a3 = r4 * 256u + 16u * (c16 ^ (12 + r4));
      st.b0 = r4 * 256u + 16u * (c16 ^ (r4 << 1)); st.b1 = r4 * 256u + 16u * (c16 ^ ((4 + r4) << 1));
    } else {
      st.sA = __builtin_amdgcn_make_buffer_rsrc((void*)(br ? wqB : wqA), (short)0, DM * NH * DH * 2, 0x00020000);
      st.sB = __builtin_amdgcn_make_buffer_rsrc((void*)(br ? woB : woA), (short)0, DM * NH * DH * 2, 0x00020000);
      st.a0 = r4 * 8192u + 16u * (c16 ^ (r4 << 1)); st.a1 = r4 * 8192u + 16u * (c16 ^ ((4 + r4) << 1));
      st.a2 = 0; st.a3 = 0;
      st.b0 = r4 * 1024u + 16u * (c16 ^ (r4 << 1)); st.b1 = r4 * 1024u + 16u * (c16 ^ ((4 + r4) << 1));
    }
    const int ns = (w < 2) ? NKV : NW;
    for (int k = 0; k < ns; ++k) stream_issue(st);
    f32x4 acc[4][2][4];
#pragma unroll
    for (int m = 0; m < 4; ++m)
#pragma unroll
      for (int e = 0; e < 2; ++e)
#pragma unroll
        for (int q = 0; q < 4; ++q) acc[m][e][q] = Z4;
    bf16x8 of[4];
#define BPHASE(USE)                                                                               \
    do {                                                                                          \
      const bool use_ = (USE);                                                                    \
      if (use_) wait_vm(st.ki - 1 - st.kc);                                                       \
      BAR();                                                                                      \
      if (st.ki < 128 && st.ki < st.kc + ns) stream_issue(st);                                    \
      if (use_) st.kc++;                                                                          \
    } while (0)
#define OPROJ(P, TOFF)                                                                            \
    do {                                                                                          \
      if (((P) & 3) == 0) {                                                                       \
        _Pragma("unroll") for (int q = 0; q < 4; ++q) of[q] = lds_b128(L0 + OB_OFF + (q * 4 + ((P) >> 2)) * 1024 + lane * 16); \
      }                                                                                           \
      _Pragma("unroll") for (int e_ = 0; e_ < 2; ++e_) {                                          \
        const bf16x8 a = tr_frag<4096>((trP ^ (32u * (2 * w + e_))) + (TOFF));                    \
        _Pragma("unroll") for (int q = 0; q < 4; ++q) acc[(P) & 3][e_][q] = MFMA(a, of[q], acc[(P) & 3][e_][q]); \
      }                                                                                           \
    } while (0)
#pragma unroll
    for (int e = 0; e < 8; ++e) BPHASE(w >= 2);
    for (int h = 0; h < HPG; ++h) {
      const bool use = (w < 2) || (w == 2 ? h < HPG - 1 : h >= 1);
#pragma unroll
      for (int p = 0; p < 16; ++p) {
        BPHASE(use);

        if (h >= 1) OPROJ(p, W3_OFF + (p & 3) * 8192);
      }
    }
#pragma unroll
    for (int e = 0; e < 8; ++e) {
      BPHASE(w >= 2);
      OPROJ(2 * e, W2_OFF + (e & 3) * 8192);
      OPROJ(2 * e + 1, W3_OFF + (e & 3) * 8192);
    }
    float* pw = part + (size_t)blockIdx.x * 32768 + (size_t)w * 8192 + lane * 4;
#pragma unroll
    for (int m = 0; m < 4; ++m)
#pragma unroll
      for (int e = 0; e < 2; ++e)
#pragma unroll
        for (int q = 0; q < 4; ++q) *(f32x4*)(pw + ((m * 2 + e) * 4 + q) * 256) = acc[m][e][q];
  }
}

__global__ __launch_bounds__(256) void reduce_kernel(const float* __restrict__ part, __hip_bfloat16* __restrict__ out) {
  const int t = blockIdx.x * 256 + threadIdx.x;
  const int u = t & 8191, bb = t >> 13, br = bb & 1, b = bb >> 1;
  const float* p = part + (size_t)(b * 8 + br * 4) * 32768 + u * 4;
  const f32x4 a0 = *(const f32x4*)p, a1 = *(const f32x4*)(p + 32768), a2 = *(const f32x4*)(p + 65536), a3 = *(const f32x4*)(p + 98304);
  const f32x4 s = (a0 + a1) + (a2 + a3);
  const int lane = u & 63, qt = (u >> 6) & 3, e = (u >> 8) & 1, m = (u >> 9) & 3, w = u >> 11;
  const int q = 16 * qt + (lane & 15), n = 128 * m + 32 * w + 16 * e + 4 * (lane >> 4);
  u32x2 r = {pk2(s[0], s[1]), pk2(s[2], s[3])};
  *(u32x2*)(out + ((size_t)b * SQ + q) * (2 * DM) + br * DM + n) = r;
}

extern "C" void kernel_launch(void* const* d_in, const int* in_sizes, int n_in, void* d_out, int out_size, void* d_ws,
                              size_t ws_size, hipStream_t stream) {
  const __hip_bfloat16* const* in = (const __hip_bfloat16* const*)d_in;
  float* part = (float*)d_ws;
  fused_kernel<<<256, 512, 0, stream>>>(in[0], in[1], in[2], in[3], in[4], in[5], in[6], in[7], in[8], in[9], part);
  reduce_kernel<<<2048, 256, 0, stream>>>(part, (__hip_bfloat16*)d_out);
}
```

```cpp
#include <hip/hip_runtime.h>
#include <hip/hip_bf16.h>
#include <stdint.h>

typedef short bf16x8 __attribute__((ext_vector_type(8)));
typedef short s16x4 __attribute__((ext_vector_type(4)));
typedef float f32x4 __attribute__((ext_vector_type(4)));
typedef float f32x2 __attribute__((ext_vector_type(2)));
typedef unsigned u32x4 __attribute__((ext_vector_type(4)));
typedef unsigned u32x2 __attribute__((ext_vector_type(2)));
typedef __bf16 bf16x2_t __attribute__((ext_vector_type(2)));
#define LAS __attribute__((address_space(3)))
#define DI __device__ __forceinline__

constexpr int NB = 32, SQ = 64, DM = 512, NH = 32, SK = 512, DH = 128, HPG = 8;
constexpr int K_OFF = 0, V_OFF = 32768, WQ_OFF = 65536, WO_OFF = 98304, OB_OFF = 131072;
constexpr int DUM_OFF = OB_OFF + 16384;
constexpr int LDS_BYTES = DUM_OFF + 8192;
static_assert(LDS_BYTES <= 163840, "LDS");
#define XPOL "nt"
#define WNT false
constexpr float THR = 8.f;
constexpr float QSCALE = 0.08838834764831845f * 1.4426950408889634f;

#define Z4 (f32x4{0.f, 0.f, 0.f, 0.f})
#define MF_ACC(d, a, b) asm volatile("v_mfma_f32_16x16x32_bf16 %0, %1, %2, %0" : "+v"(d) : "v"(a), "v"(b))
#define MF_ZERO(d, a, b) asm volatile("v_mfma_f32_16x16x32_bf16 %0, %1, %2, 0" : "=v"(d) : "v"(a), "v"(b))
#define MF_C(d, a, b, c) asm volatile("v_mfma_f32_16x16x32_bf16 %0, %1, %2, %3" : "=v"(d) : "v"(a), "v"(b), "v"(c))
#define MF(first, d, a, b) do { if (first) MF_ZERO(d, a, b); else MF_ACC(d, a, b); } while (0)
#define NOPV(x) asm volatile("s_nop 1" : "+v"(x))
#define NOPV2(x, y) asm volatile("s_nop 1" : "+v"(x), "+v"(y))
#define NOPD2(x, y) asm volatile("s_nop 7" : "+v"(x), "+v"(y))

DI unsigned pk2(float a, float b) { f32x2 v = {a, b}; return __builtin_bit_cast(unsigned, __builtin_convertvector(v, bf16x2_t)); }
DI bf16x8 pack8(const f32x4& a, const f32x4& b) {
  u32x4 w = {pk2(a[0], a[1]), pk2(a[2], a[3]), pk2(b[0], b[1]), pk2(b[2], b[3])};
  return __builtin_bit_cast(bf16x8, w);
}
DI bf16x8 lds_b128(unsigned addr) { return *(const LAS bf16x8*)(uintptr_t)addr; }
DI s16x4 lds_tr(unsigned addr) { return __builtin_amdgcn_ds_read_tr16_b64_v4i16((LAS s16x4*)(uintptr_t)addr); }
template <int USTEP> DI bf16x8 tr_frag(unsigned a) {
  s16x4 lo = lds_tr(a), hi = lds_tr(a + USTEP);
  return __builtin_shufflevector(lo, hi, 0, 1, 2, 3, 4, 5, 6, 7);
}
DI float fmax3(float a, float b, float c) { return fmaxf(fmaxf(a, b), c); }
DI float xmax16_32(float v) {
  auto r = __builtin_amdgcn_permlane16_swap(__float_as_uint(v), __float_as_uint(v), false, false);
  v = fmaxf(__uint_as_float(r[0]), __uint_as_float(r[1]));
  auto q = __builtin_amdgcn_permlane32_swap(__float_as_uint(v), __float_as_uint(v), false, false);
  return fmaxf(__uint_as_float(q[0]), __uint_as_float(q[1]));
}
#define STR2(x) #x
#define STR(x) STR2(x)
#define BAR() asm volatile("s_waitcnt lgkmcnt(0)\n\ts_barrier" ::: "memory")
#define WAITBAR(N) asm volatile("s_waitcnt vmcnt(" STR(N) ") lgkmcnt(0)\n\ts_barrier" ::: "memory")
#define SB() __builtin_amdgcn_sched_barrier(0)
#define OPAQUE(x) asm volatile("" : "+v"(x))

#define DMA8(NTS)                                                                                   \
  asm volatile("s_nop 1\n\ts_mov_b32 %0, m0\n\ts_mov_b32 m0, %7\n\ts_mov_b32 %1, %8\n\t"            \
               "buffer_load_dwordx4 %2, %6, %1 offen " NTS "lds\n\t"                                \
               "s_add_u32 m0, m0, 0x400\n\ts_add_u32 %1, %1, %9\n\t"                                \
               "buffer_load_dwordx4 %3, %6, %1 offen " NTS "lds\n\t"                                \
               "s_add_u32 m0, m0, 0x400\n\ts_add_u32 %1, %1, %9\n\t"                                \
               "buffer_load_dwordx4 %4, %6, %1 offen " NTS "lds\n\t"                                \
               "s_add_u32 m0, m0, 0x400\n\ts_add_u32 %1, %1, %9\n\t"                                \
               "buffer_load_dwordx4 %5, %6, %1 offen " NTS "lds\n\t"                                \
               "s_add_u32 m0, m0, 0x400\n\ts_add_u32 %1, %1, %9\n\t"                                \
               "buffer_load_dwordx4 %2, %6, %1 offen " NTS "lds\n\t"                                \
               "s_add_u32 m0, m0, 0x400\n\ts_add_u32 %1, %1, %9\n\t"                                \
               "buffer_load_dwordx4 %3, %6, %1 offen " NTS "lds\n\t"                                \
               "s_add_u32 m0, m0, 0x400\n\ts_add_u32 %1, %1, %9\n\t"                                \
               "buffer_load_dwordx4 %4, %6, %1 offen " NTS "lds\n\t"                                \
               "s_add_u32 m0, m0, 0x400\n\ts_add_u32 %1, %1, %9\n\t"                                \
               "buffer_load_dwordx4 %5, %6, %1 offen " NTS "lds\n\t"                                \
               "s_mov_b32 m0, %0"                                                                   \
               : "=&s"(keep), "=&s"(tmp)                                                            \
               : "v"(v0), "v"(v1), "v"(v2), "v"(v3), "s"(srd), "s"(lds), "s"(so), "s"(ps)           \
               : "memory", "scc")
template <bool NT>
DI void dma8(const char* base, unsigned nrec, unsigned vb, unsigned m1, unsigned m2, unsigned m3, unsigned lds, unsigned so, unsigned ps) {
  unsigned keep, tmp;
  const __amdgpu_buffer_rsrc_t srd = __builtin_amdgcn_make_buffer_rsrc((void*)base, (short)0, nrec, 0x00020000);
  const unsigned v0 = vb, v1 = vb ^ m1, v2 = vb ^ m2, v3 = vb ^ m3;
  lds = __builtin_amdgcn_readfirstlane(lds); so = __builtin_amdgcn_readfirstlane(so); ps = __builtin_amdgcn_readfirstlane(ps);
  if (NT) DMA8("nt "); else DMA8("");
}

__global__ __launch_bounds__(768, 3) void fused_kernel(
    const __hip_bfloat16* __restrict__ xA, const __hip_bfloat16* __restrict__ kA, const __hip_bfloat16* __restrict__ vA,
    const __hip_bfloat16* __restrict__ wqA, const __hip_bfloat16* __restrict__ woA,
    const __hip_bfloat16* __restrict__ xB, const __hip_bfloat16* __restrict__ kB, const __hip_bfloat16* __restrict__ vB,
    const __hip_bfloat16* __restrict__ wqB, const __hip_bfloat16* __restrict__ woB, u32x4* __restrict__ part) {
  __shared__ __attribute__((aligned(1024))) char smem[LDS_BYTES];
  const unsigned L0 = (unsigned)(uintptr_t)(LAS char*)smem;
  const int tid = threadIdx.x;
  const int wave = __builtin_amdgcn_readfirstlane(tid >> 6), w = wave & 3;
  const int grp = blockIdx.x & 7, b = blockIdx.x >> 3, br = grp >> 2, hg = grp & 3, hbase = hg * HPG;
#define LANEVARS                                                                                  \
  int lane = tid & 63; OPAQUE(lane);                                                              \
  const int i16 = lane & 15, g = lane >> 4, qq = i16 >> 2, pp = i16 & 3;                          \
  const unsigned rho = 4 * (g & 1) + qq;

  if (wave < 4) {
    LANEVARS
    unsigned ka[4];
#pragma unroll
    for (int s = 0; s < 4; ++s) ka[s] = L0 + K_OFF + 256u * i16 + 16u * ((4 * s + g) ^ i16);
    unsigned tv = L0 + V_OFF + 256u * (4 * g + qq) + 32u * rho + 8u * pp;
    unsigned obw = L0 + OB_OFF + w * 1024 + lane * 16; OPAQUE(obw);
    bf16x8 ones = {0x3f80, 0x3f80, 0x3f80, 0x3f80, 0x3f80, 0x3f80, 0x3f80, 0x3f80};
    bf16x8 qf[4], fk[8], fv[8], pf;
    f32x4 sc[2], o[8], ol;
    float nm;
#define KF(c, SO) lds_b128(ka[(c) & 3] + (SO) + 4096u * ((c) >> 2))
#define VF(c, SO) tr_frag<4096>((tv ^ (32u * (c))) + (SO))
#define QLOAD() _Pragma("unroll") for (int s = 0; s < 4; ++s) qf[s] = lds_b128(obw + s * 4096)
#define SOFTMAX(FIRST)                                                                            \
    do {                                                                                          \
      NOPD2(sc[0], sc[1]);                                                                        \
      float mx = fmax3(fmax3(sc[0][0], sc[0][1], sc[0][2]), fmax3(sc[0][3], sc[1][0], sc[1][1]), fmaxf(sc[1][2], sc[1][3])); \
      if (FIRST) {                                                                                \
        mx = xmax16_32(mx); nm = -mx; sc[0] -= mx; sc[1] -= mx;                                   \
      } else if (__builtin_expect(__any(mx > THR), 0)) {     \
        const float d = fmaxf(xmax16_32(mx), 0.f), alpha = __builtin_amdgcn_exp2f(-d);            \
        nm -= d; sc[0] -= d; sc[1] -= d;                                                          \
        _Pragma("unroll") for (int c = 0; c < 8; ++c) o[c] *= alpha;                              \
        ol *= alpha;                                                                              \
      }                                                                                           \
      _Pragma("unroll") for (int r = 0; r < 4; ++r) { sc[0][r] = __builtin_amdgcn_exp2f(sc[0][r]); sc[1][r] = __builtin_amdgcn_exp2f(sc[1][r]); } \
      pf = pack8(sc[0], sc[1]);                                                                   \
    } while (0)
#pragma unroll
    for (int e = 0; e < 9; ++e) BAR();
#pragma unroll
    for (int c = 0; c < 8; ++c) fk[c] = KF(c, 0);
    BAR();
    QLOAD();
#pragma unroll
    for (int c = 0; c < 8; ++c) fv[c] = VF(c, 8192);
    SB();
#pragma unroll
    for (int c = 0; c < 8; ++c) { MF((c & 3) == 0, sc[c >> 2], fk[c], qf[c & 3]); fk[c] = KF(c, 8192); SB(); }
    SOFTMAX(true);
#pragma nounroll
    for (int h = 0; h < HPG; ++h) {
#pragma unroll
      for (int p = 0; p < 16; ++p) {
        const unsigned so = ((p + 2) & 3) * 8192;
        BAR();
        OPAQUE(tv);
        if (p == 15) { QLOAD(); SB(); }
        NOPV2(pf, ones);
        MF(p == 0, ol, ones, pf);
#pragma unroll
        for (int c = 0; c < 8; ++c) { MF(p == 0, o[c], fv[c], pf); fv[c] = VF(c, so); SB(); }
        f32x4 cinit = {nm, nm, nm, nm};
        if (p != 15) NOPV(cinit);
#pragma unroll
        for (int c = 0; c < 8; ++c) {
          if ((c & 3) == 0 && p != 15) MF_C(sc[c >> 2], fk[c], qf[c & 3], cinit); else MF((c & 3) == 0, sc[c >> 2], fk[c], qf[c & 3]);
          fk[c] = KF(c, so);
          SB();
        }
        if (p == 15) {
          OPAQUE(ol);
#pragma unroll
          for (int c = 0; c < 8; ++c) OPAQUE(o[c]);
          const float inv = 1.f / ol[0];
#pragma unroll
          for (int s = 0; s < 4; ++s) *(LAS bf16x8*)(uintptr_t)(obw + s * 4096) = pack8(o[2 * s] * inv, o[2 * s + 1] * inv);
        }
        SOFTMAX(p == 15);
      }
    }
#pragma unroll
    for (int e = 0; e < 8; ++e) BAR();
  } else if (wave < 8) {
    LANEVARS
    const int odd = w & 1;
    bf16x8 xf[16];
    {
      const char* xr = (const char*)((br ? xB : xA) + ((size_t)b * SQ + 16 * w + i16) * DM) + 16 * g;
#pragma unroll
      for (int s = 0; s < 16; ++s) asm volatile("global_load_dwordx4 %0, %1, off offset:%2 " XPOL : "=v"(xf[s]) : "v"(xr), "i"(64 * s) : "memory");
    }
    const size_t kvoff = ((size_t)b * NH + hbase) * SK * DH;
    const char* pKV = (const char*)((odd ? (br ? vB : vA) : (br ? kB : kA)) + kvoff);
    const char* pWQ = (const char*)(br ? wqB : wqA);
    const char* pWO = (const char*)(br ? woB : woA);
    const unsigned bx = 16u * (i16 ^ (g << 1));
    const unsigned vkv = 256u * g + (odd ? bx : 16u * (i16 ^ g)), vwq = 8192u * g + bx, vwo = 1024u * g + bx;
#define KVNONE false
#define WNONE false
#define KV_ISSUE(T, DUMMY)                                                                        \
    do {                                                                                          \
      const int T_ = (T); const unsigned n_ = (unsigned)(T_ - 8 - odd);                           \
      const bool none_ = (DUMMY) || n_ >= 128u || KVNONE;                                         \
      dma8<true>(pKV, HPG * SK * DH * 2, vkv, odd ? 128u : 64u, odd ? 0u : 128u, odd ? 128u : 192u,     \
           L0 + (none_ ? DUM_OFF : (odd ? V_OFF : K_OFF) + (T_ & 3) * 8192), none_ ? 0x40000000u : n_ * 8192u, 1024u); \
    } while (0)
#define W_ISSUE(T)                                                                                \
    do {                                                                                          \
      const int T_ = (T);                                                                         \
      const int km = T_ - 8 - 17 * odd;                                  \
      int h_ = (km >> 4) + 1 - odd, t_ = km & 15;                                                 \
      bool isq_ = !odd, none_ = km < 0 || km >= 112;                                              \
      if (T_ < 8) { isq_ = true; h_ = 0; t_ = 2 * T_ + odd; none_ = false; }                      \
      if (T_ >= 138) { isq_ = false; h_ = 7; t_ = 2 * (T_ - 138) + odd; none_ = T_ >= 146; }      \
      none_ = none_ || WNONE;                                                                     \
      const unsigned soq_ = (unsigned)t_ * (32u * 8192u) + (unsigned)(hbase + h_) * 256u;         \
      const unsigned soo_ = (unsigned)((hbase + h_) * 128 + 32 * (t_ >> 2)) * 1024u + (unsigned)(t_ & 3) * 256u; \
      dma8<WNT>(isq_ ? pWQ : pWO, DM * NH * DH * 2, isq_ ? vwq : vwo, isq_ ? 0u : 128u, isq_ ? 128u : 0u, 128u, \
           L0 + (none_ ? DUM_OFF : (odd ? WO_OFF : WQ_OFF) + (T_ & 3) * 8192),                    \
           none_ ? 0x40000000u : (isq_ ? soq_ : soo_), isq_ ? 32768u : 4096u);                    \
    } while (0)
#define ISSUE(P, PAR)                                                                             \
    do {                                                                                          \
      if (w >= 2) W_ISSUE((P) + 3);                                                               \
      else KV_ISSUE((P) == 0 ? 11 : (P) + 3, (P) >= 1 && (P) <= 8);                               \
    } while (0)
#define LWAITBAR() WAITBAR(16)
#pragma nounroll
    for (int p = 0; p < 3; ++p) { if (w >= 2) W_ISSUE(p); else KV_ISSUE(8 + p, false); }
    asm volatile("s_waitcnt vmcnt(24)" : "+v"(xf[0]), "+v"(xf[1]), "+v"(xf[2]), "+v"(xf[3]), "+v"(xf[4]), "+v"(xf[5]), "+v"(xf[6]), "+v"(xf[7]),
                 "+v"(xf[8]), "+v"(xf[9]), "+v"(xf[10]), "+v"(xf[11]), "+v"(xf[12]), "+v"(xf[13]), "+v"(xf[14]), "+v"(xf[15]) :: "memory");
    unsigned tq = L0 + WQ_OFF + 256u * (8 * g + qq) + 32u * rho + 8u * pp; OPAQUE(tq);
    unsigned qw = L0 + OB_OFF + w * 1024 + (i16 + 16 * (g >> 1)) * 16 + 8 * (g & 1); OPAQUE(qw);
    f32x4 qacc[8];
    bf16x8 fa[8];
#define WQF(c, TOFF) tr_frag<1024>((tq ^ (32u * (c))) + (TOFF))
#define QFIN()                                                                                    \
    NOPD2(qacc[6], qacc[7]);                                                                      \
    _Pragma("unroll") for (int c = 0; c < 8; ++c) {                                               \
      OPAQUE(qacc[c]);                                                                            \
      const f32x4 a = qacc[c] * QSCALE;                                                           \
      u32x2 v = {pk2(a[0], a[1]), pk2(a[2], a[3])};                                               \
      *(LAS u32x2*)(uintptr_t)(qw + (c >> 1) * 4096 + (c & 1) * 512) = v;                         \
    }
#pragma unroll
    for (int e = 0; e < 8; ++e) {
      LWAITBAR();
      OPAQUE(tq);
#pragma unroll
      for (int c = 0; c < 8; ++c) fa[c] = WQF(c, (e & 3) * 8192);
      SB();
      ISSUE(e, e & 1);
      SB();
#pragma unroll
      for (int c = 0; c < 8; ++c) {
        MF(e == 0, qacc[c], fa[c], xf[2 * e]);
        fa[c] = WQF(c, (WO_OFF - WQ_OFF) + (e & 3) * 8192);
        SB();
      }
#pragma unroll
      for (int c = 0; c < 8; ++c) MF_ACC(qacc[c], fa[c], xf[2 * e + 1]);
    }
    QFIN();
    LWAITBAR();
#pragma unroll
    for (int c = 0; c < 8; ++c) fa[c] = WQF(c, 0);
    ISSUE(8, 0);
#pragma nounroll
    for (int h = 0; h < HPG - 1; ++h) {
#pragma unroll
      for (int j = 0; j < 16; ++j) {
        const int P = 9 + 16 * h + j;
        LWAITBAR();
        OPAQUE(tq);
#pragma unroll
        for (int c = 0; c < 8; ++c) {
          MF(j == 0, qacc[c], fa[c], xf[j]);
          fa[c] = WQF(c, ((j + 1) & 3) * 8192);
          SB();
        }
        ISSUE(P, (j + 1) & 1);
        if (j == 15) { QFIN(); }
      }
    }
#pragma nounroll
    for (int P = 121; P < 145; P += 2) {
      LWAITBAR();
      ISSUE(P, 1);
      LWAITBAR();
      ISSUE(P + 1, 0);
    }
    LWAITBAR();
    asm volatile("s_waitcnt vmcnt(0)" ::: "memory");
  } else {
    LANEVARS
    f32x4 acc[4][2][4];
#pragma unroll
    for (int m = 0; m < 4; ++m)
#pragma unroll
      for (int e = 0; e < 2; ++e)
#pragma unroll
        for (int q = 0; q < 4; ++q) acc[m][e][q] = Z4;
    bf16x8 of[4];
    const unsigned tw = L0 + WQ_OFF + 256u * (4 * g + qq) + 32u * rho + 8u * pp;
    unsigned tw0 = tw ^ (32u * (2 * w)), tw1 = tw ^ (32u * (2 * w + 1)), ob = L0 + OB_OFF + lane * 16;
    asm volatile("" : "+v"(tw0), "+v"(tw1), "+v"(ob));
#define OLOAD(S) _Pragma("unroll") for (int q = 0; q < 4; ++q) of[q] = lds_b128(ob + (S) * 4096 + q * 1024)
#define WOF(T, TOFF) tr_frag<4096>((T) + (TOFF))
#define OPROJ(M, A0, A1)                                                                          \
    do {                                                                                          \
      _Pragma("unroll") for (int q = 0; q < 4; ++q) MF_ACC(acc[M][0][q], A0, of[q]);             \
      _Pragma("unroll") for (int q = 0; q < 4; ++q) MF_ACC(acc[M][1][q], A1, of[q]);             \
    } while (0)
#pragma nounroll
    for (int e = 0; e < 26; ++e) BAR();
    bf16x8 f0 = WOF(tw0, (WO_OFF - WQ_OFF) + 8192), f1 = WOF(tw1, (WO_OFF - WQ_OFF) + 8192);
#pragma nounroll
    for (int it = 0; it < 28; ++it) {
#pragma unroll
      for (int m = 0; m < 4; ++m) {
        BAR();
        if (m == 0) { OLOAD(it & 3); }
#pragma unroll
        for (int q = 0; q < 4; ++q) MF_ACC(acc[m][0][q], f0, of[q]);
        f0 = WOF(tw0, (WO_OFF - WQ_OFF) + ((m + 2) & 3) * 8192);
        SB();
#pragma unroll
        for (int q = 0; q < 4; ++q) MF_ACC(acc[m][1][q], f1, of[q]);
        f1 = WOF(tw1, (WO_OFF - WQ_OFF) + ((m + 2) & 3) * 8192);
        SB();
      }
    }
#pragma nounroll
    for (int e2 = 0; e2 < 4; ++e2) {
#pragma unroll
      for (int u = 0; u < 2; ++u) {
        BAR();
        if (u == 0) { OLOAD(e2); }
        const unsigned so = (unsigned)(((2 * e2 + u + 2) & 3) * 8192);
        f0 = WOF(tw0, so); f1 = WOF(tw1, so);
        OPROJ(2 * u, f0, f1);
        f0 = WOF(tw0, (WO_OFF - WQ_OFF) + so); f1 = WOF(tw1, (WO_OFF - WQ_OFF) + so);
        OPROJ(2 * u + 1, f0, f1);
      }
    }
    u32x4* pw = part + (size_t)blockIdx.x * 4096 + w * 1024 + lane;
    asm volatile("s_nop 7");
#pragma unroll
    for (int m = 0; m < 4; ++m)
#pragma unroll
      for (int q = 0; q < 4; ++q) { OPAQUE(acc[m][0][q]); OPAQUE(acc[m][1][q]); }
#pragma unroll
    for (int m = 0; m < 4; ++m)
#pragma unroll
      for (int q = 0; q < 4; ++q) {
        pw[(m * 4 + q) * 64] = __builtin_bit_cast(u32x4, pack8(acc[m][0][q], acc[m][1][q]));
      }
  }
}

DI float blo(unsigned u) { return __uint_as_float(u << 16); }
DI float bhi(unsigned u) { return __uint_as_float(u & 0xffff0000u); }
__global__ __launch_bounds__(256) void reduce_kernel(const u32x4* __restrict__ part, __hip_bfloat16* __restrict__ out) {
  const int t = blockIdx.x * 256 + threadIdx.x;
  const int u = t & 4095, bb = t >> 12, br = bb & 1, b = bb >> 1;
  const u32x4* p = part + (size_t)(b * 8 + br * 4) * 4096 + u;
  const u32x4 a0 = p[0], a1 = p[4096], a2 = p[8192], a3 = p[12288];
  unsigned r[4];
#pragma unroll
  for (int i = 0; i < 4; ++i)
    r[i] = pk2((blo(a0[i]) + blo(a1[i])) + (blo(a2[i]) + blo(a3[i])), (bhi(a0[i]) + bhi(a1[i])) + (bhi(a2[i]) + bhi(a3[i])));
  const int lane = u & 63, qt = (u >> 6) & 3, m = (u >> 8) & 3, w = u >> 10;
  const int q = 16 * qt + (lane & 15), n = 128 * m + 32 * w + 4 * (lane >> 4);
  __hip_bfloat16* o = out + ((size_t)b * SQ + q) * (2 * DM) + br * DM + n;
  *(u32x2*)o = u32x2{r[0], r[1]};
  *(u32x2*)(o + 16) = u32x2{r[2], r[3]};
}

extern "C" void kernel_launch(void* const* d_in, const int* in_sizes, int n_in, void* d_out, int out_size, void* d_ws,
                              size_t ws_size, hipStream_t stream) {
  const __hip_bfloat16* const* in = (const __hip_bfloat16* const*)d_in;
  u32x4* part = (u32x4*)d_ws;
  fused_kernel<<<256, 768, 0, stream>>>(in[0], in[1], in[2], in[3], in[4], in[5], in[6], in[7], in[8], in[9], part);
  reduce_kernel<<<1024, 256, 0, stream>>>(part, (__hip_bfloat16*)d_out);
}
```

```cpp
#include <hip/hip_runtime.h>
#include <hip/hip_bf16.h>
#include <stdint.h>

typedef short bf16x8 __attribute__((ext_vector_type(8)));
typedef short s16x4 __attribute__((ext_vector_type(4)));
typedef float f32x4 __attribute__((ext_vector_type(4)));
typedef float f32x2 __attribute__((ext_vector_type(2)));
typedef unsigned u32x4 __attribute__((ext_vector_type(4)));
typedef unsigned u32x2 __attribute__((ext_vector_type(2)));
typedef __bf16 bf16x2_t __attribute__((ext_vector_type(2)));
#define LAS __attribute__((address_space(3)))
#define DI __device__ __forceinline__

constexpr int NB = 32, SQ = 64, DM = 512, NH = 32, SK = 512, DH = 128, HPG = 8;
constexpr int K_OFF = 0, V_OFF = 32768, WQ_OFF = 65536, WO_OFF = 98304, OB_OFF = 131072;
constexpr int HS_OFF = OB_OFF + 16384;
constexpr int DUM_OFF = HS_OFF + 1024;
constexpr int LDS_BYTES = DUM_OFF + 8192;
static_assert(LDS_BYTES <= 163840, "LDS");
#define XPOL "nt"
#define WNT false
#define KVMSK 0xffffu
#define KVNT true
#define KVLIM 128
#define WLIM(x) (x)
constexpr float THR = 8.f;
constexpr float QSCALE = 0.08838834764831845f * 1.4426950408889634f;

#define Z4 (f32x4{0.f, 0.f, 0.f, 0.f})
#define MF_ACC(d, a, b) asm volatile("v_mfma_f32_16x16x32_bf16 %0, %1, %2, %0" : "+v"(d) : "v"(a), "v"(b))
#define MF_ZERO(d, a, b) asm volatile("v_mfma_f32_16x16x32_bf16 %0, %1, %2, 0" : "=v"(d) : "v"(a), "v"(b))
#define MF_C(d, a, b, c) asm volatile("v_mfma_f32_16x16x32_bf16 %0, %1, %2, %3" : "=v"(d) : "v"(a), "v"(b), "v"(c))
#define MF(first, d, a, b) do { if (first) MF_ZERO(d, a, b); else MF_ACC(d, a, b); } while (0)
#define NOPV(x) asm volatile("s_nop 1" : "+v"(x))
#define NOPV2(x, y) asm volatile("s_nop 1" : "+v"(x), "+v"(y))
#define NOPD2(x, y) asm volatile("s_nop 7" : "+v"(x), "+v"(y))

DI unsigned pk2(float a, float b) { f32x2 v = {a, b}; return __builtin_bit_cast(unsigned, __builtin_convertvector(v, bf16x2_t)); }
DI bf16x8 pack8(const f32x4& a, const f32x4& b) {
  u32x4 w = {pk2(a[0], a[1]), pk2(a[2], a[3]), pk2(b[0], b[1]), pk2(b[2], b[3])};
  return __builtin_bit_cast(bf16x8, w);
}
DI bf16x8 lds_b128(unsigned addr) { return *(const LAS bf16x8*)(uintptr_t)addr; }
DI s16x4 lds_tr(unsigned addr) { return __builtin_amdgcn_ds_read_tr16_b64_v4i16((LAS s16x4*)(uintptr_t)addr); }
template <int USTEP> DI bf16x8 tr_frag(unsigned a) {
  s16x4 lo = lds_tr(a), hi = lds_tr(a + USTEP);
  return __builtin_shufflevector(lo, hi, 0, 1, 2, 3, 4, 5, 6, 7);
}
DI float fmax3(float a, float b, float c) { return fmaxf(fmaxf(a, b), c); }
DI float xmax16_32(float v) {
  auto r = __builtin_amdgcn_permlane16_swap(__float_as_uint(v), __float_as_uint(v), false, false);
  v = fmaxf(__uint_as_float(r[0]), __uint_as_float(r[1]));
  auto q = __builtin_amdgcn_permlane32_swap(__float_as_uint(v), __float_as_uint(v), false, false);
  return fmaxf(__uint_as_float(q[0]), __uint_as_float(q[1]));
}
#define STR2(x) #x
#define STR(x) STR2(x)
#define BAR() asm volatile("s_waitcnt lgkmcnt(0)\n\ts_barrier" ::: "memory")
#define WAITBAR(N) asm volatile("s_waitcnt vmcnt(" STR(N) ") lgkmcnt(0)\n\ts_barrier" ::: "memory")
#define SB() __builtin_amdgcn_sched_barrier(0)
#define OPAQUE(x) asm volatile("" : "+v"(x))
DI int fresh_lane() { int l; asm volatile("v_mbcnt_lo_u32_b32 %0, -1, 0\n\tv_mbcnt_hi_u32_b32 %0, -1, %0" : "=v"(l)); return l; }

#define DMA8(POL)                                                                                 \
  asm volatile("s_mov_b32 m0, %0\n\ts_add_u32 %0, %0, 0x1000\n\t"                                 \
               "buffer_load_dwordx4 %2, %6, %1 offen " POL "lds\n\t"                              \
               "buffer_load_dwordx4 %3, %6, %1 offen offset:1024 " POL "lds\n\t"                  \
               "buffer_load_dwordx4 %4, %6, %1 offen offset:2048 " POL "lds\n\t"                  \
               "buffer_load_dwordx4 %5, %6, %1 offen offset:3072 " POL "lds\n\t"                  \
               "s_mov_b32 m0, %0\n\ts_add_u32 %1, %1, %7\n\t"                                     \
               "buffer_load_dwordx4 %2, %6, %1 offen " POL "lds\n\t"                              \
               "buffer_load_dwordx4 %3, %6, %1 offen offset:1024 " POL "lds\n\t"                  \
               "buffer_load_dwordx4 %4, %6, %1 offen offset:2048 " POL "lds\n\t"                  \
               "buffer_load_dwordx4 %5, %6, %1 offen offset:3072 " POL "lds"                      \
               : "+s"(lds), "+s"(so)                                                              \
               : "v"(st.v0), "v"(st.v1), "v"(st.v2), "v"(st.v3), "s"(st.srd), "s"(st.ps4) : "memory", "scc")
struct Stream { __amdgpu_buffer_rsrc_t srd; unsigned v0, v1, v2, v3, ps4, t0, lim, s0, r1, m1, s1, s2, r2, m2, s3, mul, base; bool nt; };
#define RFL(x) ((unsigned)__builtin_amdgcn_readfirstlane((int)(x)))
DI void stream_set(Stream& st, int kind, const void* p, unsigned nrec, int lane, unsigned t0, unsigned lim, unsigned s0, unsigned r1,
                   unsigned m1, unsigned s1, unsigned s2, unsigned r2, unsigned m2, unsigned s3, unsigned mul, unsigned base, bool nt) {
  const unsigned g = lane >> 4, c = lane & 15, pitch = kind < 2 ? 256u : kind == 2 ? 8192u : 1024u;
  const unsigned vb = pitch * g + 16u * (c ^ (kind == 0 ? g : g << 1)), d = 4u * pitch - 1024u;
  st.v0 = vb;
  st.v1 = (vb ^ (kind == 0 ? 64u : kind == 2 ? 0u : 128u)) + d;
  st.v2 = (vb ^ ((kind & 1) ? 0u : 128u)) + 2u * d;
  st.v3 = (vb ^ (kind == 0 ? 192u : 128u)) + 3u * d;
  const unsigned long long pp = (unsigned long long)p;
  const unsigned long long pu = ((unsigned long long)RFL((unsigned)(pp >> 32)) << 32) | (unsigned long long)RFL((unsigned)pp);
  st.srd = __builtin_amdgcn_make_buffer_rsrc((void*)pu, (short)0, RFL(nrec), 0x00020000);
  st.ps4 = RFL(16u * pitch); st.t0 = RFL(t0); st.lim = RFL(lim); st.s0 = RFL(s0); st.r1 = RFL(r1); st.m1 = RFL(m1); st.s1 = RFL(s1);
  st.s2 = RFL(s2); st.r2 = RFL(r2); st.m2 = RFL(m2); st.s3 = RFL(s3); st.mul = RFL(mul); st.base = RFL(base); st.nt = nt;
  asm volatile("s_nop 4" : "+s"(st.srd), "+s"(st.ps4));
}
DI void stream_issue(const Stream& st, int T, unsigned ring, unsigned dummy) {
  const unsigned km = (unsigned)T - st.t0;
  const bool ok = km < st.lim;
  unsigned so = ok ? ((((km << st.s0) + st.r1) & st.m1) << st.s1) + ((((km >> st.s2) + st.r2) & st.m2) << st.s3) + (km >> 4) * st.mul + st.base : 0x40000000u;
  unsigned lds = ok ? ring + ((unsigned)T & 3u) * 8192u : dummy;
  if (st.nt) DMA8("nt "); else DMA8("");
}

typedef __attribute__((address_space(1))) unsigned gu32;
DI unsigned ld_agent(const unsigned* p) { return __hip_atomic_load((const gu32*)p, __ATOMIC_RELAXED, __HIP_MEMORY_SCOPE_AGENT); }
DI void st_agent(unsigned* p, unsigned v) { __hip_atomic_store((gu32*)p, v, __ATOMIC_RELAXED, __HIP_MEMORY_SCOPE_AGENT); }
#define VMWAIT() asm volatile("s_waitcnt vmcnt(0)" ::: "memory")
DI float blo(unsigned u) { return __uint_as_float(u << 16); }
DI float bhi(unsigned u) { return __uint_as_float(u & 0xffff0000u); }
constexpr size_t XBUF_UNITS = (size_t)64 * 16 * 1024;
constexpr int SPIN_MAX = 1 << 22;

__global__ __launch_bounds__(768, 3) void fused_kernel(
    const __hip_bfloat16* __restrict__ xA, const __hip_bfloat16* __restrict__ kA, const __hip_bfloat16* __restrict__ vA,
    const __hip_bfloat16* __restrict__ wqA, const __hip_bfloat16* __restrict__ woA,
    const __hip_bfloat16* __restrict__ xB, const __hip_bfloat16* __restrict__ kB, const __hip_bfloat16* __restrict__ vB,
    const __hip_bfloat16* __restrict__ wqB, const __hip_bfloat16* __restrict__ woB, u32x4* __restrict__ part,
    __hip_bfloat16* __restrict__ out) {
  __shared__ __attribute__((aligned(1024))) char smem[LDS_BYTES];
  const unsigned L0 = (unsigned)(uintptr_t)(LAS char*)smem;
  const int tid = threadIdx.x;
  const int wave = __builtin_amdgcn_readfirstlane(tid >> 6), w = wave & 3;
  const int grp = blockIdx.x & 7, b = blockIdx.x >> 3, br = grp >> 2, hg = grp & 3, hbase = hg * HPG;
  const unsigned rq = b & 15, rs = b & 3;
  const unsigned rm = (hg + 1) & 3;
#define LANEVARS                                                                                  \
  int lane = tid & 63; OPAQUE(lane);                                                              \
  const int i16 = lane & 15, g = lane >> 4, qq = i16 >> 2, pp = i16 & 3;                          \
  const unsigned rho = 4 * (g & 1) + qq;

  if (wave < 4) {
    LANEVARS
    unsigned ka[4];
#pragma unroll
    for (int s = 0; s < 4; ++s) ka[s] = L0 + K_OFF + 256u * i16 + 16u * ((4 * s + g) ^ i16);
    unsigned tv = L0 + V_OFF + 256u * (4 * g + qq) + 32u * rho + 8u * pp;
    unsigned obw = L0 + OB_OFF + w * 1024 + lane * 16; OPAQUE(obw);
    bf16x8 ones = {0x3f80, 0x3f80, 0x3f80, 0x3f80, 0x3f80, 0x3f80, 0x3f80, 0x3f80};
    bf16x8 qf[4], fk[8], fv[8], pf;
    f32x4 sc[2], o[8], ol;
    float nm;
#define KF(c, SO) lds_b128(ka[(c) & 3] + (SO) + 4096u * ((c) >> 2))
#define VF(c, SO) tr_frag<4096>((tv ^ (32u * (c))) + (SO))
#define QLOAD() _Pragma("unroll") for (int s = 0; s < 4; ++s) qf[s] = lds_b128(obw + s * 4096)
#define SOFTMAX(FIRST)                                                                            \
    do {                                                                                          \
      NOPD2(sc[0], sc[1]);                                                                        \
      float mx = fmax3(fmax3(sc[0][0], sc[0][1], sc[0][2]), fmax3(sc[0][3], sc[1][0], sc[1][1]), fmaxf(sc[1][2], sc[1][3])); \
      if (FIRST) {                                                                                \
        mx = xmax16_32(mx); nm = -mx; sc[0] -= mx; sc[1] -= mx;                                   \
      } else if (__builtin_expect(__any(mx > THR), 0)) {     \
        const float d = fmaxf(xmax16_32(mx), 0.f), alpha = __builtin_amdgcn_exp2f(-d);            \
        nm -= d; sc[0] -= d; sc[1] -= d;                                                          \
        _Pragma("unroll") for (int c = 0; c < 8; ++c) o[c] *= alpha;                              \
        ol *= alpha;                                                                              \
      }                                                                                           \
      _Pragma("unroll") for (int r = 0; r < 4; ++r) { sc[0][r] = __builtin_amdgcn_exp2f(sc[0][r]); sc[1][r] = __builtin_amdgcn_exp2f(sc[1][r]); } \
      pf = pack8(sc[0], sc[1]);                                                                   \
    } while (0)
#pragma unroll
    for (int e = 0; e < 5; ++e) BAR();
#pragma unroll
    for (int c = 0; c < 8; ++c) fk[c] = KF(c, 0);
    BAR();
    QLOAD();
#pragma unroll
    for (int c = 0; c < 8; ++c) fv[c] = VF(c, 8192);
    SB();
#pragma unroll
    for (int c = 0; c < 8; ++c) { MF((c & 3) == 0, sc[c >> 2], fk[c], qf[c & 3]); fk[c] = KF(c, 8192); SB(); }
    SOFTMAX(true);
#pragma nounroll
    for (int h = 0; h < HPG; ++h) {
#pragma unroll
      for (int p = 0; p < 16; ++p) {
        const unsigned so = ((p + 2) & 3) * 8192;
        BAR();
        OPAQUE(tv);
        if (p == 15) { QLOAD(); SB(); }
        f32x4 cinit = {nm, nm, nm, nm};
#define PVBLK()                                                                                   \
        NOPV2(pf, ones);                                                                          \
        MF(p == 0, ol, ones, pf);                                                                 \
        _Pragma("unroll") for (int c = 0; c < 8; ++c) { MF(p == 0, o[c], fv[c], pf); fv[c] = VF(c, so); SB(); }
#define QKBLK()                                                                                   \
        if (p != 15) NOPV(cinit);                                                                 \
        _Pragma("unroll") for (int c = 0; c < 8; ++c) {                                           \
          if ((c & 3) == 0 && p != 15) MF_C(sc[c >> 2], fk[c], qf[c & 3], cinit); else MF((c & 3) == 0, sc[c >> 2], fk[c], qf[c & 3]); \
          fk[c] = KF(c, so);                                                                      \
          SB();                                                                                   \
        }
        PVBLK(); QKBLK();
        if (p == 15) {
          OPAQUE(ol);
#pragma unroll
          for (int c = 0; c < 8; ++c) OPAQUE(o[c]);
          const float inv = 1.f / ol[0];
#pragma unroll
          for (int s = 0; s < 4; ++s) *(LAS bf16x8*)(uintptr_t)(obw + s * 4096) = pack8(o[2 * s] * inv, o[2 * s + 1] * inv);
        }
        SOFTMAX(p == 15);
      }
    }
    BAR(); BAR();
  } else if (wave < 8) {
    LANEVARS
    unsigned* hs = (unsigned*)(part + XBUF_UNITS) + (size_t)(blockIdx.x & ~3) * 128;
    const int pj = lane & 3;
    const bool hsl = w == 0 && lane < 4 && pj != hg;
    unsigned nonce = 0, pn = 0;
    unsigned e0 = 0, d0 = 0;
    if (hsl) { e0 = ld_agent(hs + pj * 128 + 32 + hg); d0 = ld_agent(hs + pj * 128 + 64 + hg); }
    bf16x8 xf[16];
    {
      const char* xr = (const char*)((br ? xB : xA) + ((size_t)b * SQ + 16 * w + i16) * DM) + 16 * g;
#pragma unroll
      for (int s = 0; s < 16; ++s) xf[s] = __builtin_nontemporal_load((const bf16x8*)(xr + 64 * ((s + rq) & 15)));
    }
    unsigned tq[8];
#pragma unroll
    for (int c = 0; c < 8; ++c) { tq[c] = (L0 + WQ_OFF + 256u * (8 * g + qq) + 32u * rho + 8u * pp) ^ (32u * c); OPAQUE(tq[c]); }
    unsigned qw = L0 + OB_OFF + w * 1024 + (i16 + 16 * (g >> 1)) * 16 + 8 * (g & 1); OPAQUE(qw);
    f32x4 qacc[8];
    bf16x8 fa[8];
#define WQF(c, TOFF) tr_frag<1024>(tq[c] + (TOFF))
#define QFIN()                                                                                    \
    NOPD2(qacc[6], qacc[7]);                                                                      \
    _Pragma("unroll") for (int c = 0; c < 8; ++c) {                                               \
      OPAQUE(qacc[c]);                                                                            \
      const f32x4 a = qacc[c] * QSCALE;                                                           \
      u32x2 v = {pk2(a[0], a[1]), pk2(a[2], a[3])};                                               \
      *(LAS u32x2*)(uintptr_t)(qw + (c >> 1) * 4096 + (c & 1) * 512) = v;                         \
    }
#pragma unroll
    for (int s = 0; s < 16; ++s) {
      if ((s & 3) == 0) BAR();
#pragma unroll
      for (int c = 0; c < 8; ++c) {
        if (s > 0) MF(s == 1, qacc[c], fa[c], xf[s - 1]);
        fa[c] = WQF(c, ((s & 3) - 2) * 32768 + (s >> 2) * 8192);
        SB();
      }
    }
    BAR();
#pragma unroll
    for (int c = 0; c < 8; ++c) { MF_ACC(qacc[c], fa[c], xf[15]); fa[c] = WQF(c, 0); SB(); }
    QFIN();
    if (w == 0) {
      const unsigned long long be = __ballot((~e0 >> (2 * pj)) & 1u), bd = __ballot((~d0 >> (2 * pj + 1)) & 1u);
#pragma unroll
      for (int j = 0; j < 4; ++j) nonce |= (unsigned)((be >> j) & 1ull) << (2 * j) | (unsigned)((bd >> j) & 1ull) << (2 * j + 1);
      if (lane == 0) { st_agent(hs + hg * 128, nonce); *(LAS unsigned*)(uintptr_t)(L0 + HS_OFF) = nonce; }
    }
#pragma nounroll
    for (int h = 0; h < HPG - 1; ++h) {
      if (h == 1 && hsl) pn = ld_agent(hs + pj * 128);
      if (h == 2) { VMWAIT(); if (hsl) st_agent(hs + hg * 128 + 32 + pj, pn); }
#pragma unroll
      for (int j = 0; j < 16; ++j) {
        BAR();
#pragma unroll
        for (int c = 0; c < 8; ++c) {
          MF(j == 0, qacc[c], fa[c], xf[j]);
          fa[c] = WQF(c, ((j + 1) & 3) * 8192);
          SB();
        }
        if (j == 15) { QFIN(); }
      }
    }
    bool known = !hsl;
    {
      unsigned e = 0;
      if (hsl) e = ld_agent(hs + pj * 128 + 32 + hg);
      BAR(); BAR(); BAR(); BAR();
      VMWAIT();
      if (hsl) pn = ld_agent(hs + pj * 128);
      BAR(); BAR(); BAR(); BAR();
      if (hsl) { st_agent(hs + hg * 128 + 32 + pj, pn); known = e == nonce; }
    }
#pragma nounroll
    for (int P = 125; P < 136; ++P) BAR();
    if (w == 0) {
      for (int sp = 0; sp < SPIN_MAX && !__all(known); ++sp) {
        if (!known) {
          const unsigned e = ld_agent(hs + pj * 128 + 32 + hg);
          VMWAIT();
          pn = ld_agent(hs + pj * 128);
          st_agent(hs + hg * 128 + 32 + pj, pn);
          known = e == nonce;
        }
        __builtin_amdgcn_s_sleep(4);
      }
      if (hsl) st_agent(hs + hg * 128 + 64 + pj, pn);
    }
  } else {
    LANEVARS
    const void* pwq = br ? wqB : wqA;
    const void* pwo = br ? woB : woA;
    const unsigned ring = L0 + 32768u * w, dummy = L0 + DUM_OFF;
    Stream st;
    stream_set(st, 2, pwq, DM * NH * DH * 2, lane, 0, WLIM(4), 2, w + rq, 15, 18, 0, 0, 0, 0, 0, hbase * 256u, WNT);
    stream_issue(st, 0, ring, dummy); stream_issue(st, 1, ring, dummy); stream_issue(st, 2, ring, dummy);
    f32x4 acc[4][2][4];
#pragma unroll
    for (int m = 0; m < 4; ++m)
#pragma unroll
      for (int e = 0; e < 2; ++e)
#pragma unroll
        for (int q = 0; q < 4; ++q) acc[m][e][q] = Z4;
    bf16x8 of[4];
    const unsigned tw = L0 + WQ_OFF + 256u * (4 * g + qq) + 32u * rho + 8u * pp;
    unsigned tw0 = tw ^ (32u * (2 * w)), tw1 = tw ^ (32u * (2 * w + 1)), ob = L0 + OB_OFF + lane * 16;
    asm volatile("" : "+v"(tw0), "+v"(tw1), "+v"(ob));
#define OLOAD(S) _Pragma("unroll") for (int q = 0; q < 4; ++q) of[q] = lds_b128(ob + ((((S) + rs) & 3) << 12) + q * 1024)
#define WOF(T, TOFF) tr_frag<4096>((T) + (TOFF))
    WAITBAR(16);
    stream_issue(st, 3, ring, dummy);
    {
      const size_t kvoff = ((size_t)b * NH + hbase) * SK * DH;
      if (w < 2) stream_set(st, w, (w ? (br ? vB : vA) : (br ? kB : kA)) + kvoff, HPG * SK * DH * 2, lane, 4 + w, KVLIM, 0, 0, 0, 0, 0, 0, KVMSK, 13, 0, 0, KVNT);
      else if (w == 2) stream_set(st, 2, pwq, DM * NH * DH * 2, lane, 4, WLIM(112), 0, rq, 15, 18, 0, 0, 0, 0, 256u, (hbase + 1) * 256u, WNT);
      else stream_set(st, 3, pwo, DM * NH * DH * 2, lane, 21, WLIM(112), 0, rm, 3, 8, 2, rs, 3, 15, 131072u, hbase * 131072u, WNT);
    }
#pragma nounroll
    for (int P = 1; P < 22; ++P) { WAITBAR(16); stream_issue(st, P + 3, ring, dummy); }
    bf16x8 f0 = WOF(tw0, (WO_OFF - WQ_OFF) + 8192), f1 = WOF(tw1, (WO_OFF - WQ_OFF) + 8192);
#pragma nounroll
    for (int it = 0; it < 28; ++it) {
      if (it == 27)
        stream_set(st, 3, pwo, DM * NH * DH * 2, fresh_lane(), 133, WLIM(4), 0, rm, 3, 8, 0, 0, 0, 0, 0, (hbase + 7) * 131072u + w * 32768u, WNT);
#pragma unroll
      for (int m = 0; m < 4; ++m) {
        WAITBAR(16);
        if (m == 0) { OLOAD(it & 3); }
        stream_issue(st, 25 + 4 * it + m, ring, dummy);
#pragma unroll
        for (int q = 0; q < 4; ++q) MF_ACC(acc[m][0][q], f0, of[q]);
        f0 = WOF(tw0, (WO_OFF - WQ_OFF) + ((m + 2) & 3) * 8192);
        SB();
#pragma unroll
        for (int q = 0; q < 4; ++q) MF_ACC(acc[m][1][q], f1, of[q]);
        f1 = WOF(tw1, (WO_OFF - WQ_OFF) + ((m + 2) & 3) * 8192);
        SB();
      }
    }
    WAITBAR(0);
    const int ln = fresh_lane();
    const __amdgpu_buffer_rsrc_t sx = __builtin_amdgcn_make_buffer_rsrc((void*)(part + (size_t)(blockIdx.x >> 2) * 16384), (short)0, 16384 * 16, 0x00020000);
#pragma unroll
    for (int m = 0; m < 4; ++m) {
#pragma unroll
      for (int s = 0; s < 4; ++s) {
#pragma unroll
        for (int q = 0; q < 4; ++q) of[q] = lds_b128(ob + s * 4096 + q * 1024);
        f0 = WOF(tw0, (s - 2) * 32768 + ((m + 1) & 3) * 8192); f1 = WOF(tw1, (s - 2) * 32768 + ((m + 1) & 3) * 8192);
#pragma unroll
        for (int q = 0; q < 4; ++q) MF_ACC(acc[m][0][q], f0, of[q]);
#pragma unroll
        for (int q = 0; q < 4; ++q) MF_ACC(acc[m][1][q], f1, of[q]);
      }
      asm volatile("s_nop 7");
#pragma unroll
      for (int q = 0; q < 4; ++q) { OPAQUE(acc[m][0][q]); OPAQUE(acc[m][1][q]); }
      if (m < 3) {
        const unsigned so = ((((m + rm) & 3) * 4 + hg) * 1024 + w * 256) * 16;
#pragma unroll
        for (int q = 0; q < 4; ++q)
          __builtin_amdgcn_raw_buffer_store_b128(__builtin_bit_cast(u32x4, pack8(acc[m][0][q], acc[m][1][q])), sx, ln * 16 + q * 1024, so, 16);
        asm volatile("s_nop 1" ::: "memory");
        SB();
      }
    }
    WAITBAR(0);
    {
      const unsigned nonce = *(const LAS unsigned*)(uintptr_t)(L0 + HS_OFF);
      const unsigned* hs = (const unsigned*)(part + XBUF_UNITS) + (size_t)(blockIdx.x & ~3) * 128;
      const int pj = ln & 3;
      for (int sp = 0; sp < SPIN_MAX; ++sp) {
        const unsigned d = ld_agent(hs + pj * 128 + 64 + hg);
        if (__all(pj == hg || d == nonce)) break;
        __builtin_amdgcn_s_sleep(1);
      }
      __builtin_amdgcn_fence(__ATOMIC_ACQUIRE, "wavefront");
      u32x4 r[3][4];
#pragma unroll
      for (int j = 0; j < 3; ++j)
#pragma unroll
        for (int q = 0; q < 4; ++q)
          r[j][q] = __builtin_amdgcn_raw_buffer_load_b128(sx, ln * 16 + q * 1024, ((hg * 4 + ((hg + 1 + j) & 3)) * 1024 + w * 256) * 16, 16);
      const int g2 = ln >> 4;
      __hip_bfloat16* op = out + ((size_t)b * SQ + (ln & 15)) * (2 * DM) + br * DM + hg * 128 + w * 32 + (g2 & 1) * 16 + (g2 >> 1) * 8;
#pragma unroll
      for (int q = 0; q < 4; ++q) {
        unsigned v[4];
#pragma unroll
        for (int i = 0; i < 4; ++i) {
          const f32x4& a = acc[3][i >> 1][q];
          v[i] = pk2((a[(i & 1) * 2] + blo(r[0][q][i])) + (blo(r[1][q][i]) + blo(r[2][q][i])),
                     (a[(i & 1) * 2 + 1] + bhi(r[0][q][i])) + (bhi(r[1][q][i]) + bhi(r[2][q][i])));
        }
        auto s0 = __builtin_amdgcn_permlane16_swap(v[0], v[2], false, false);
        auto s1 = __builtin_amdgcn_permlane16_swap(v[1], v[3], false, false);
        *(u32x4*)(op + (size_t)q * 16 * (2 * DM)) = u32x4{s0[0], s1[0], s0[1], s1[1]};
      }
    }
  }
}

extern "C" void kernel_launch(void* const* d_in, const int* in_sizes, int n_in, void* d_out, int out_size, void* d_ws,
                              size_t ws_size, hipStream_t stream) {
  const __hip_bfloat16* const* in = (const __hip_bfloat16* const*)d_in;
  u32x4* part = (u32x4*)d_ws;
  fused_kernel<<<256, 768, 0, stream>>>(in[0], in[1], in[2], in[3], in[4], in[5], in[6], in[7], in[8], in[9], part, (__hip_bfloat16*)d_out);
}
```

```cpp
#include <hip/hip_runtime.h>
#include <hip/hip_bf16.h>
#include <stdint.h>

typedef short bf16x8 __attribute__((ext_vector_type(8)));
typedef short s16x4 __attribute__((ext_vector_type(4)));
typedef float f32x4 __attribute__((ext_vector_type(4)));
typedef float f32x2 __attribute__((ext_vector_type(2)));
typedef unsigned u32x4 __attribute__((ext_vector_type(4)));
typedef unsigned u32x2 __attribute__((ext_vector_type(2)));
typedef __bf16 bf16x2_t __attribute__((ext_vector_type(2)));
#define LAS __attribute__((address_space(3)))
#define DI __device__ __forceinline__

constexpr int NB = 32, SQ = 64, DM = 512, NH = 32, SK = 512, DH = 128, HPG = 8;
constexpr int K_OFF = 0, V_OFF = 32768, WQ_OFF = 65536, WO_OFF = 98304, OB_OFF = 131072;
constexpr int DUM_OFF = OB_OFF + 16384;
constexpr int LDS_BYTES = DUM_OFF + 8192;
static_assert(LDS_BYTES <= 163840, "LDS");
#define XPOL "nt"
#define WNT false
#define KVMSK 0xffffu
#define KVNT true
#define KVLIM 128
#define WLIM(x) (x)
constexpr float THR = 8.f;
constexpr float QSCALE = 0.08838834764831845f * 1.4426950408889634f;

#define Z4 (f32x4{0.f, 0.f, 0.f, 0.f})
#define MF_ACC(d, a, b) asm volatile("v_mfma_f32_16x16x32_bf16 %0, %1, %2, %0" : "+v"(d) : "v"(a), "v"(b))
#define MF_ZERO(d, a, b) asm volatile("v_mfma_f32_16x16x32_bf16 %0, %1, %2, 0" : "=v"(d) : "v"(a), "v"(b))
#define MF_C(d, a, b, c) asm volatile("v_mfma_f32_16x16x32_bf16 %0, %1, %2, %3" : "=v"(d) : "v"(a), "v"(b), "v"(c))
#define MF(first, d, a, b) do { if (first) MF_ZERO(d, a, b); else MF_ACC(d, a, b); } while (0)
#define NOPV(x) asm volatile("s_nop 1" : "+v"(x))
#define NOPV2(x, y) asm volatile("s_nop 1" : "+v"(x), "+v"(y))
#define NOPD2(x, y) asm volatile("s_nop 7" : "+v"(x), "+v"(y))

DI unsigned pk2(float a, float b) { f32x2 v = {a, b}; return __builtin_bit_cast(unsigned, __builtin_convertvector(v, bf16x2_t)); }
DI bf16x8 pack8(const f32x4& a, const f32x4& b) {
  u32x4 w = {pk2(a[0], a[1]), pk2(a[2], a[3]), pk2(b[0], b[1]), pk2(b[2], b[3])};
  return __builtin_bit_cast(bf16x8, w);
}
DI bf16x8 lds_b128(unsigned addr) { return *(const LAS bf16x8*)(uintptr_t)addr; }
DI s16x4 lds_tr(unsigned addr) { return __builtin_amdgcn_ds_read_tr16_b64_v4i16((LAS s16x4*)(uintptr_t)addr); }
template <int USTEP> DI bf16x8 tr_frag(unsigned a) {
  s16x4 lo = lds_tr(a), hi = lds_tr(a + USTEP);
  return __builtin_shufflevector(lo, hi, 0, 1, 2, 3, 4, 5, 6, 7);
}
DI float fmax3(float a, float b, float c) { return fmaxf(fmaxf(a, b), c); }
DI float xmax16_32(float v) {
  auto r = __builtin_amdgcn_permlane16_swap(__float_as_uint(v), __float_as_uint(v), false, false);
  v = fmaxf(__uint_as_float(r[0]), __uint_as_float(r[1]));
  auto q = __builtin_amdgcn_permlane32_swap(__float_as_uint(v), __float_as_uint(v), false, false);
  return fmaxf(__uint_as_float(q[0]), __uint_as_float(q[1]));
}
#define STR2(x) #x
#define STR(x) STR2(x)
#define BAR() asm volatile("s_waitcnt lgkmcnt(0)\n\ts_barrier" ::: "memory")
#define WAITBAR(N) asm volatile("s_waitcnt vmcnt(" STR(N) ") lgkmcnt(0)\n\ts_barrier" ::: "memory")
#define SB() __builtin_amdgcn_sched_barrier(0)
#define OPAQUE(x) asm volatile("" : "+v"(x))
DI int fresh_lane() { int l; asm volatile("v_mbcnt_lo_u32_b32 %0, -1, 0\n\tv_mbcnt_hi_u32_b32 %0, -1, %0" : "=v"(l)); return l; }

#define DMA8(POL)                                                                                 \
  asm volatile("s_mov_b32 m0, %0\n\ts_add_u32 %0, %0, 0x1000\n\t"                                 \
               "buffer_load_dwordx4 %2, %6, %1 offen " POL "lds\n\t"                              \
               "buffer_load_dwordx4 %3, %6, %1 offen offset:1024 " POL "lds\n\t"                  \
               "buffer_load_dwordx4 %4, %6, %1 offen offset:2048 " POL "lds\n\t"                  \
               "buffer_load_dwordx4 %5, %6, %1 offen offset:3072 " POL "lds\n\t"                  \
               "s_mov_b32 m0, %0\n\ts_add_u32 %1, %1, %7\n\t"                                     \
               "buffer_load_dwordx4 %2, %6, %1 offen " POL "lds\n\t"                              \
               "buffer_load_dwordx4 %3, %6, %1 offen offset:1024 " POL "lds\n\t"                  \
               "buffer_load_dwordx4 %4, %6, %1 offen offset:2048 " POL "lds\n\t"                  \
               "buffer_load_dwordx4 %5, %6, %1 offen offset:3072 " POL "lds"                      \
               : "+s"(lds), "+s"(so)                                                              \
               : "v"(st.v0), "v"(st.v1), "v"(st.v2), "v"(st.v3), "s"(st.srd), "s"(st.ps4) : "memory", "scc")
struct Stream { __amdgpu_buffer_rsrc_t srd; unsigned v0, v1, v2, v3, ps4, t0, lim, s0, r1, m1, s1, s2, r2, m2, s3, mul, base; bool nt; };
#define RFL(x) ((unsigned)__builtin_amdgcn_readfirstlane((int)(x)))
DI void stream_set(Stream& st, int kind, const void* p, unsigned nrec, int lane, unsigned t0, unsigned lim, unsigned s0, unsigned r1,
                   unsigned m1, unsigned s1, unsigned s2, unsigned r2, unsigned m2, unsigned s3, unsigned mul, unsigned base, bool nt) {
  const unsigned g = lane >> 4, c = lane & 15, pitch = kind < 2 ? 256u : kind == 2 ? 8192u : 1024u;
  const unsigned vb = pitch * g + 16u * (c ^ (kind == 0 ? g : g << 1)), d = 4u * pitch - 1024u;
  st.v0 = vb;
  st.v1 = (vb ^ (kind == 0 ? 64u : kind == 2 ? 0u : 128u)) + d;
  st.v2 = (vb ^ ((kind & 1) ? 0u : 128u)) + 2u * d;
  st.v3 = (vb ^ (kind == 0 ? 192u : 128u)) + 3u * d;
  const unsigned long long pp = (unsigned long long)p;
  const unsigned long long pu = ((unsigned long long)RFL((unsigned)(pp >> 32)) << 32) | (unsigned long long)RFL((unsigned)pp);
  st.srd = __builtin_amdgcn_make_buffer_rsrc((void*)pu, (short)0, RFL(nrec), 0x00020000);
  st.ps4 = RFL(16u * pitch); st.t0 = RFL(t0); st.lim = RFL(lim); st.s0 = RFL(s0); st.r1 = RFL(r1); st.m1 = RFL(m1); st.s1 = RFL(s1);
  st.s2 = RFL(s2); st.r2 = RFL(r2); st.m2 = RFL(m2); st.s3 = RFL(s3); st.mul = RFL(mul); st.base = RFL(base); st.nt = nt;
  asm volatile("s_nop 4" : "+s"(st.srd), "+s"(st.ps4));
}
DI void stream_issue(const Stream& st, int T, unsigned ring, unsigned dummy) {
  const unsigned km = (unsigned)T - st.t0;
  const bool ok = km < st.lim;
  unsigned so = ok ? ((((km << st.s0) + st.r1) & st.m1) << st.s1) + ((((km >> st.s2) + st.r2) & st.m2) << st.s3) + (km >> 4) * st.mul + st.base : 0x40000000u;
  unsigned lds = ok ? ring + ((unsigned)T & 3u) * 8192u : dummy;
  if (st.nt) DMA8("nt "); else DMA8("");
}

__global__ __launch_bounds__(768, 3) void fused_kernel(
    const __hip_bfloat16* __restrict__ xA, const __hip_bfloat16* __restrict__ kA, const __hip_bfloat16* __restrict__ vA,
    const __hip_bfloat16* __restrict__ wqA, const __hip_bfloat16* __restrict__ woA,
    const __hip_bfloat16* __restrict__ xB, const __hip_bfloat16* __restrict__ kB, const __hip_bfloat16* __restrict__ vB,
    const __hip_bfloat16* __restrict__ wqB, const __hip_bfloat16* __restrict__ woB, u32x4* __restrict__ part) {
  __shared__ __attribute__((aligned(1024))) char smem[LDS_BYTES];
  const unsigned L0 = (unsigned)(uintptr_t)(LAS char*)smem;
  const int tid = threadIdx.x;
  const int wave = __builtin_amdgcn_readfirstlane(tid >> 6), w = wave & 3;
  const int grp = blockIdx.x & 7, b = blockIdx.x >> 3, br = grp >> 2, hg = grp & 3, hbase = hg * HPG;
  const unsigned rq = b & 15, rs = b & 3, rm = (b >> 2) & 3;
#define LANEVARS                                                                                  \
  int lane = tid & 63; OPAQUE(lane);                                                              \
  const int i16 = lane & 15, g = lane >> 4, qq = i16 >> 2, pp = i16 & 3;                          \
  const unsigned rho = 4 * (g & 1) + qq;

  if (wave < 4) {
    LANEVARS
    unsigned ka[4];
#pragma unroll
    for (int s = 0; s < 4; ++s) ka[s] = L0 + K_OFF + 256u * i16 + 16u * ((4 * s + g) ^ i16);
    unsigned tv = L0 + V_OFF + 256u * (4 * g + qq) + 32u * rho + 8u * pp;
    unsigned obw = L0 + OB_OFF + w * 1024 + lane * 16; OPAQUE(obw);
    bf16x8 ones = {0x3f80, 0x3f80, 0x3f80, 0x3f80, 0x3f80, 0x3f80, 0x3f80, 0x3f80};
    bf16x8 qf[4], fk[8], fv[8], pf;
    f32x4 sc[2], o[8], ol;
    float nm;
#define KF(c, SO) lds_b128(ka[(c) & 3] + (SO) + 4096u * ((c) >> 2))
#define VF(c, SO) tr_frag<4096>((tv ^ (32u * (c))) + (SO))
#define QLOAD() _Pragma("unroll") for (int s = 0; s < 4; ++s) qf[s] = lds_b128(obw + s * 4096)
#define SOFTMAX(FIRST)                                                                            \
    do {                                                                                          \
      NOPD2(sc[0], sc[1]);                                                                        \
      float mx = fmax3(fmax3(sc[0][0], sc[0][1], sc[0][2]), fmax3(sc[0][3], sc[1][0], sc[1][1]), fmaxf(sc[1][2], sc[1][3])); \
      if (FIRST) {                                                                                \
        mx = xmax16_32(mx); nm = -mx; sc[0] -= mx; sc[1] -= mx;                                   \
      } else if (__builtin_expect(__any(mx > THR), 0)) {     \
        const float d = fmaxf(xmax16_32(mx), 0.f), alpha = __builtin_amdgcn_exp2f(-d);            \
        nm -= d; sc[0] -= d; sc[1] -= d;                                                          \
        _Pragma("unroll") for (int c = 0; c < 8; ++c) o[c] *= alpha;                              \
        ol *= alpha;                                                                              \
      }                                                                                           \
      _Pragma("unroll") for (int r = 0; r < 4; ++r) { sc[0][r] = __builtin_amdgcn_exp2f(sc[0][r]); sc[1][r] = __builtin_amdgcn_exp2f(sc[1][r]); } \
      pf = pack8(sc[0], sc[1]);                                                                   \
    } while (0)
#pragma unroll
    for (int e = 0; e < 5; ++e) BAR();
#pragma unroll
    for (int c = 0; c < 8; ++c) fk[c] = KF(c, 0);
    BAR();
    QLOAD();
#pragma unroll
    for (int c = 0; c < 8; ++c) fv[c] = VF(c, 8192);
    SB();
#pragma unroll
    for (int c = 0; c < 8; ++c) { MF((c & 3) == 0, sc[c >> 2], fk[c], qf[c & 3]); fk[c] = KF(c, 8192); SB(); }
    SOFTMAX(true);
#pragma nounroll
    for (int h = 0; h < HPG; ++h) {
#pragma unroll
      for (int p = 0; p < 16; ++p) {
        const unsigned so = ((p + 2) & 3) * 8192;
        BAR();
        OPAQUE(tv);
        if (p == 15) { QLOAD(); SB(); }
        f32x4 cinit = {nm, nm, nm, nm};
#define PVBLK()                                                                                   \
        NOPV2(pf, ones);                                                                          \
        MF(p == 0, ol, ones, pf);                                                                 \
        _Pragma("unroll") for (int c = 0; c < 8; ++c) { MF(p == 0, o[c], fv[c], pf); fv[c] = VF(c, so); SB(); }
#define QKBLK()                                                                                   \
        if (p != 15) NOPV(cinit);                                                                 \
        _Pragma("unroll") for (int c = 0; c < 8; ++c) {                                           \
          if ((c & 3) == 0 && p != 15) MF_C(sc[c >> 2], fk[c], qf[c & 3], cinit); else MF((c & 3) == 0, sc[c >> 2], fk[c], qf[c & 3]); \
          fk[c] = KF(c, so);                                                                      \
          SB();                                                                                   \
        }
        PVBLK(); QKBLK();
        if (p == 15) {
          OPAQUE(ol);
#pragma unroll
          for (int c = 0; c < 8; ++c) OPAQUE(o[c]);
          const float inv = 1.f / ol[0];
#pragma unroll
          for (int s = 0; s < 4; ++s) *(LAS bf16x8*)(uintptr_t)(obw + s * 4096) = pack8(o[2 * s] * inv, o[2 * s + 1] * inv);
        }
        SOFTMAX(p == 15);
      }
    }
#pragma unroll
    for (int e = 0; e < 3; ++e) BAR();
  } else if (wave < 8) {
    LANEVARS
    bf16x8 xf[16];
    {
      const char* xr = (const char*)((br ? xB : xA) + ((size_t)b * SQ + 16 * w + i16) * DM) + 16 * g;
#pragma unroll
      for (int s = 0; s < 16; ++s) xf[s] = __builtin_nontemporal_load((const bf16x8*)(xr + 64 * ((s + rq) & 15)));
    }
    unsigned tq[8];
#pragma unroll
    for (int c = 0; c < 8; ++c) { tq[c] = (L0 + WQ_OFF + 256u * (8 * g + qq) + 32u * rho + 8u * pp) ^ (32u * c); OPAQUE(tq[c]); }
    unsigned qw = L0 + OB_OFF + w * 1024 + (i16 + 16 * (g >> 1)) * 16 + 8 * (g & 1); OPAQUE(qw);
    f32x4 qacc[8];
    bf16x8 fa[8];
#define WQF(c, TOFF) tr_frag<1024>(tq[c] + (TOFF))
#define QFIN()                                                                                    \
    NOPD2(qacc[6], qacc[7]);                                                                      \
    _Pragma("unroll") for (int c = 0; c < 8; ++c) {                                               \
      OPAQUE(qacc[c]);                                                                            \
      const f32x4 a = qacc[c] * QSCALE;                                                           \
      u32x2 v = {pk2(a[0], a[1]), pk2(a[2], a[3])};                                               \
      *(LAS u32x2*)(uintptr_t)(qw + (c >> 1) * 4096 + (c & 1) * 512) = v;                         \
    }
#pragma unroll
    for (int s = 0; s < 16; ++s) {
      if ((s & 3) == 0) BAR();
#pragma unroll
      for (int c = 0; c < 8; ++c) {
        if (s > 0) MF(s == 1, qacc[c], fa[c], xf[s - 1]);
        fa[c] = WQF(c, ((s & 3) - 2) * 32768 + (s >> 2) * 8192);
        SB();
      }
    }
    BAR();
#pragma unroll
    for (int c = 0; c < 8; ++c) { MF_ACC(qacc[c], fa[c], xf[15]); fa[c] = WQF(c, 0); SB(); }
    QFIN();
#pragma nounroll
    for (int h = 0; h < HPG - 1; ++h) {
#pragma unroll
      for (int j = 0; j < 16; ++j) {
        BAR();
#pragma unroll
        for (int c = 0; c < 8; ++c) {
          MF(j == 0, qacc[c], fa[c], xf[j]);
          fa[c] = WQF(c, ((j + 1) & 3) * 8192);
          SB();
        }
        if (j == 15) { QFIN(); }
      }
    }
#pragma nounroll
    for (int P = 117; P < 137; ++P) BAR();
  } else {
    LANEVARS
    const void* pwq = br ? wqB : wqA;
    const void* pwo = br ? woB : woA;
    const unsigned ring = L0 + 32768u * w, dummy = L0 + DUM_OFF;
    Stream st;
    stream_set(st, 2, pwq, DM * NH * DH * 2, lane, 0, WLIM(4), 2, w + rq, 15, 18, 0, 0, 0, 0, 0, hbase * 256u, WNT);
    stream_issue(st, 0, ring, dummy); stream_issue(st, 1, ring, dummy); stream_issue(st, 2, ring, dummy);
    f32x4 acc[4][2][4];
#pragma unroll
    for (int m = 0; m < 4; ++m)
#pragma unroll
      for (int e = 0; e < 2; ++e)
#pragma unroll
        for (int q = 0; q < 4; ++q) acc[m][e][q] = Z4;
    bf16x8 of[4];
    const unsigned tw = L0 + WQ_OFF + 256u * (4 * g + qq) + 32u * rho + 8u * pp;
    unsigned tw0 = tw ^ (32u * (2 * w)), tw1 = tw ^ (32u * (2 * w + 1)), ob = L0 + OB_OFF + lane * 16;
    asm volatile("" : "+v"(tw0), "+v"(tw1), "+v"(ob));
#define OLOAD(S) _Pragma("unroll") for (int q = 0; q < 4; ++q) of[q] = lds_b128(ob + ((((S) + rs) & 3) << 12) + q * 1024)
#define WOF(T, TOFF) tr_frag<4096>((T) + (TOFF))
    WAITBAR(16);
    stream_issue(st, 3, ring, dummy);
    {
      const size_t kvoff = ((size_t)b * NH + hbase) * SK * DH;
      if (w < 2) stream_set(st, w, (w ? (br ? vB : vA) : (br ? kB : kA)) + kvoff, HPG * SK * DH * 2, lane, 4 + w, KVLIM, 0, 0, 0, 0, 0, 0, KVMSK, 13, 0, 0, KVNT);
      else if (w == 2) stream_set(st, 2, pwq, DM * NH * DH * 2, lane, 4, WLIM(112), 0, rq, 15, 18, 0, 0, 0, 0, 256u, (hbase + 1) * 256u, WNT);
      else stream_set(st, 3, pwo, DM * NH * DH * 2, lane, 21, WLIM(112), 0, rm, 3, 8, 2, rs, 3, 15, 131072u, hbase * 131072u, WNT);
    }
#pragma nounroll
    for (int P = 1; P < 22; ++P) { WAITBAR(16); stream_issue(st, P + 3, ring, dummy); }
    bf16x8 f0 = WOF(tw0, (WO_OFF - WQ_OFF) + 8192), f1 = WOF(tw1, (WO_OFF - WQ_OFF) + 8192);
#pragma nounroll
    for (int it = 0; it < 28; ++it) {
      if (it == 27)
        stream_set(st, 3, pwo, DM * NH * DH * 2, fresh_lane(), 133, WLIM(4), 0, 0, 0, 0, 0, rs, 3, 15, 0, (hbase + 7) * 131072u + ((w + rm) & 3) * 256u, WNT);
#pragma unroll
      for (int m = 0; m < 4; ++m) {
        WAITBAR(16);
        if (m == 0) { OLOAD(it & 3); }
        stream_issue(st, 25 + 4 * it + m, ring, dummy);
#pragma unroll
        for (int q = 0; q < 4; ++q) MF_ACC(acc[m][0][q], f0, of[q]);
        f0 = WOF(tw0, (WO_OFF - WQ_OFF) + ((m + 2) & 3) * 8192);
        SB();
#pragma unroll
        for (int q = 0; q < 4; ++q) MF_ACC(acc[m][1][q], f1, of[q]);
        f1 = WOF(tw1, (WO_OFF - WQ_OFF) + ((m + 2) & 3) * 8192);
        SB();
      }
    }
#pragma unroll
    for (int s = 0; s < 4; ++s) {
      if (s == 0) WAITBAR(16); else if (s == 2) WAITBAR(8); else if (s == 3) WAITBAR(0);
      OLOAD(s);
#pragma unroll
      for (int m = 0; m < 4; ++m) {
        f0 = WOF(tw0, (m - 2) * 32768 + ((s + 1) & 3) * 8192); f1 = WOF(tw1, (m - 2) * 32768 + ((s + 1) & 3) * 8192);
#pragma unroll
        for (int q = 0; q < 4; ++q) MF_ACC(acc[m][0][q], f0, of[q]);
#pragma unroll
        for (int q = 0; q < 4; ++q) MF_ACC(acc[m][1][q], f1, of[q]);
      }
    }
    u32x4* pw = part + (size_t)blockIdx.x * 4096 + w * 1024 + fresh_lane();
    asm volatile("s_nop 7");
#pragma unroll
    for (int m = 0; m < 4; ++m)
#pragma unroll
      for (int q = 0; q < 4; ++q) { OPAQUE(acc[m][0][q]); OPAQUE(acc[m][1][q]); }
#pragma unroll
    for (int m = 0; m < 4; ++m)
#pragma unroll
      for (int q = 0; q < 4; ++q) {
        pw[(((m + rm) & 3) * 4 + q) * 64] = __builtin_bit_cast(u32x4, pack8(acc[m][0][q], acc[m][1][q]));
      }
  }
}

DI float blo(unsigned u) { return __uint_as_float(u << 16); }
DI float bhi(unsigned u) { return __uint_as_float(u & 0xffff0000u); }
__global__ __launch_bounds__(256) void reduce_kernel(const u32x4* __restrict__ part, __hip_bfloat16* __restrict__ out) {
  const int t = blockIdx.x * 256 + threadIdx.x;
  const int u = t & 4095, bb = t >> 12, br = bb & 1, b = bb >> 1;
  const u32x4* p = part + (size_t)(b * 8 + br * 4) * 4096 + u;
  const u32x4 a0 = p[0], a1 = p[4096], a2 = p[8192], a3 = p[12288];
  unsigned r[4];
#pragma unroll
  for (int i = 0; i < 4; ++i)
    r[i] = pk2((blo(a0[i]) + blo(a1[i])) + (blo(a2[i]) + blo(a3[i])), (bhi(a0[i]) + bhi(a1[i])) + (bhi(a2[i]) + bhi(a3[i])));
  const int lane = u & 63, qt = (u >> 6) & 3, m = (u >> 8) & 3, w = u >> 10;
  const int q = 16 * qt + (lane & 15), n = 128 * m + 32 * w + 4 * (lane >> 4);
  __hip_bfloat16* o = out + ((size_t)b * SQ + q) * (2 * DM) + br * DM + n;
  *(u32x2*)o = u32x2{r[0], r[1]};
  *(u32x2*)(o + 16) = u32x2{r[2], r[3]};
}

extern "C" void kernel_launch(void* const* d_in, const int* in_sizes, int n_in, void* d_out, int out_size, void* d_ws,
                              size_t ws_size, hipStream_t stream) {
  const __hip_bfloat16* const* in = (const __hip_bfloat16* const*)d_in;
  u32x4* part = (u32x4*)d_ws;
  fused_kernel<<<256, 768, 0, stream>>>(in[0], in[1], in[2], in[3], in[4], in[5], in[6], in[7], in[8], in[9], part);
  reduce_kernel<<<1024, 256, 0, stream>>>(part, (__hip_bfloat16*)d_out);
}
```

```cpp
#include <hip/hip_runtime.h>
#include <hip/hip_bf16.h>
#include <stdint.h>

typedef short bf16x8 __attribute__((ext_vector_type(8)));
typedef short s16x4 __attribute__((ext_vector_type(4)));
typedef float f32x4 __attribute__((ext_vector_type(4)));
typedef float f32x2 __attribute__((ext_vector_type(2)));
typedef unsigned u32x4 __attribute__((ext_vector_type(4)));
typedef unsigned u32x2 __attribute__((ext_vector_type(2)));
typedef __bf16 bf16x2_t __attribute__((ext_vector_type(2)));
#define LAS __attribute__((address_space(3)))
#define DI __device__ __forceinline__

constexpr int SQ = 64, DM = 512, NH = 32, SK = 512, DH = 128, HPG = 8;
constexpr int K_OFF = 0, V_OFF = 32768, WQ_OFF = 65536, WO_OFF = 98304, OB_OFF = 131072;
constexpr int HS_OFF = OB_OFF + 16384;
constexpr int LDS_BYTES = HS_OFF + 1024;
static_assert(LDS_BYTES <= 163840, "LDS");
constexpr float THR = 8.f;
constexpr float QSCALE = 0.08838834764831845f * 1.4426950408889634f;

#define Z4 (f32x4{0.f, 0.f, 0.f, 0.f})
#define MF_ACC(d, a, b) asm volatile("v_mfma_f32_16x16x32_bf16 %0, %1, %2, %0" : "+v"(d) : "v"(a), "v"(b))
#define MF_ZERO(d, a, b) asm volatile("v_mfma_f32_16x16x32_bf16 %0, %1, %2, 0" : "=v"(d) : "v"(a), "v"(b))
#define MF_C(d, a, b, c) asm volatile("v_mfma_f32_16x16x32_bf16 %0, %1, %2, %3" : "=&v"(d) : "v"(a), "v"(b), "v"(c))
#define MF(first, d, a, b) do { if (first) MF_ZERO(d, a, b); else MF_ACC(d, a, b); } while (0)
#define NOPV2(x, y) asm volatile("s_nop 1" : "+v"(x), "+v"(y))
#define NOPD2(x, y) asm volatile("s_nop 7" : "+v"(x), "+v"(y))

DI unsigned pk2(float a, float b) { f32x2 v = {a, b}; return __builtin_bit_cast(unsigned, __builtin_convertvector(v, bf16x2_t)); }
DI bf16x8 pack8(const f32x4& a, const f32x4& b) {
  u32x4 w = {pk2(a[0], a[1]), pk2(a[2], a[3]), pk2(b[0], b[1]), pk2(b[2], b[3])};
  return __builtin_bit_cast(bf16x8, w);
}
DI bf16x8 lds_b128(unsigned addr) { return *(const LAS bf16x8*)(uintptr_t)addr; }
DI s16x4 lds_tr(unsigned addr) { return __builtin_amdgcn_ds_read_tr16_b64_v4i16((LAS s16x4*)(uintptr_t)addr); }
template <int USTEP> DI bf16x8 tr_frag(unsigned a) {
  s16x4 lo = lds_tr(a), hi = lds_tr(a + USTEP);
  return __builtin_shufflevector(lo, hi, 0, 1, 2, 3, 4, 5, 6, 7);
}
DI float fmax3(float a, float b, float c) { return fmaxf(fmaxf(a, b), c); }
DI float xmax16_32(float v) {
  auto r = __builtin_amdgcn_permlane16_swap(__float_as_uint(v), __float_as_uint(v), false, false);
  v = fmaxf(__uint_as_float(r[0]), __uint_as_float(r[1]));
  auto q = __builtin_amdgcn_permlane32_swap(__float_as_uint(v), __float_as_uint(v), false, false);
  return fmaxf(__uint_as_float(q[0]), __uint_as_float(q[1]));
}
#define STR2(x) #x
#define STR(x) STR2(x)
#define LGKM0() do { asm volatile("" ::: "memory"); __builtin_amdgcn_s_waitcnt(0xc07f); } while (0)
#define BAR() do { LGKM0(); asm volatile("s_barrier" ::: "memory"); } while (0)
#define WAITBAR(N) do { LGKM0(); asm volatile("s_waitcnt vmcnt(" STR(N) ")\n\ts_barrier" ::: "memory"); } while (0)
#define VMWAIT() asm volatile("s_waitcnt vmcnt(0)" ::: "memory")
#define SB() __builtin_amdgcn_sched_barrier(0)
#define OPAQUE(x) asm volatile("" : "+v"(x))
DI int fresh_lane() { int l; asm volatile("v_mbcnt_lo_u32_b32 %0, -1, 0\n\tv_mbcnt_hi_u32_b32 %0, -1, %0" : "=v"(l)); return l; }

#define DMA4(POL)                                                                                 \
  asm volatile("s_mov_b32 m0, %0\n\ts_add_u32 %0, %0, 0x1000\n\t"                                 \
               "buffer_load_dwordx4 %2, %6, %1 offen " POL "lds\n\t"                              \
               "buffer_load_dwordx4 %3, %6, %1 offen offset:1024 " POL "lds\n\t"                  \
               "buffer_load_dwordx4 %4, %6, %1 offen offset:2048 " POL "lds\n\t"                  \
               "buffer_load_dwordx4 %5, %6, %1 offen offset:3072 " POL "lds\n\t"                  \
               "s_add_u32 %1, %1, %7"                                                             \
               : "+s"(lds), "+s"(so)                                                              \
               : "v"(st.v0), "v"(st.v1), "v"(st.v2), "v"(st.v3), "s"(st.srd), "s"(st.ps4) : "memory", "scc")
struct Stream { __amdgpu_buffer_rsrc_t srd; unsigned v0, v1, v2, v3, ps4, t0, lim, s0, r1, m1, s1, s2, r2, m2, s3, mul, base, pol; };
#define RFL(x) ((unsigned)__builtin_amdgcn_readfirstlane((int)(x)))
DI void stream_set(Stream& st, int kind, const void* p, unsigned nrec, int lane, unsigned t0, unsigned lim, unsigned s0, unsigned r1,
                   unsigned m1, unsigned s1, unsigned s2, unsigned r2, unsigned m2, unsigned s3, unsigned mul, unsigned base, bool nt) {
  const unsigned g = lane >> 4, c = lane & 15, pitch = kind < 2 ? 256u : kind == 2 ? 8192u : 1024u;
  const unsigned vb = pitch * g + 16u * (c ^ (kind == 0 ? g : g << 1)), d = 4u * pitch - 1024u;
  st.v0 = vb;
  st.v1 = (vb ^ (kind == 0 ? 64u : kind == 2 ? 0u : 128u)) + d;
  st.v2 = (vb ^ ((kind & 1) ? 0u : 128u)) + 2u * d;
  st.v3 = (vb ^ (kind == 0 ? 192u : 128u)) + 3u * d;
  const unsigned long long pp = (unsigned long long)p;
  const unsigned long long pu = ((unsigned long long)RFL((unsigned)(pp >> 32)) << 32) | (unsigned long long)RFL((unsigned)pp);
  st.srd = __builtin_amdgcn_make_buffer_rsrc((void*)pu, (short)0, RFL(nrec), 0x00020000);
  st.ps4 = RFL(16u * pitch); st.t0 = RFL(t0); st.lim = RFL(lim); st.s0 = RFL(s0); st.r1 = RFL(r1); st.m1 = RFL(m1); st.s1 = RFL(s1);
  st.s2 = RFL(s2); st.r2 = RFL(r2); st.m2 = RFL(m2); st.s3 = RFL(s3); st.mul = RFL(mul); st.base = RFL(base); st.pol = RFL(nt ? 2u : 1u);
  asm volatile("s_nop 4" : "+s"(st.srd), "+s"(st.ps4));
}
DI unsigned stream_addr(const Stream& st, int T, unsigned ring, unsigned& so, unsigned& lds) {
  const unsigned km = (unsigned)T - st.t0;
  so = ((((km << st.s0) + st.r1) & st.m1) << st.s1) + ((((km >> st.s2) + st.r2) & st.m2) << st.s3) + (km >> 4) * st.mul + st.base;
  lds = ring + ((unsigned)T & 3u) * 8192u;
  return km < st.lim ? st.pol : 0u;
}
DI void stream_half(const Stream& st, unsigned& so, unsigned& lds, unsigned pol) {
  if (pol == 2) DMA4("nt "); else if (pol == 1) DMA4("");
}
DI void stream_issue(const Stream& st, int T, unsigned ring, unsigned& vb) {
  unsigned so, lds;
  const unsigned pol = stream_addr(st, T, ring, so, lds);
  stream_half(st, so, lds, pol); stream_half(st, so, lds, pol);
  vb = 2 * vb + (pol != 0);
}
#define SWAITBAR()                                                                                \
  do {                                                                                            \
    if (__builtin_expect((vb & 3u) != 3u, 0)) {                                                   \
      if (vb & 3u) asm volatile("s_waitcnt vmcnt(8)" ::: "memory"); else VMWAIT();                \
    }                                                                                             \
    WAITBAR(16);                                                                                  \
  } while (0)

typedef __attribute__((address_space(1))) unsigned gu32;
DI unsigned ld_agent(const unsigned* p) { return __hip_atomic_load((const gu32*)p, __ATOMIC_RELAXED, __HIP_MEMORY_SCOPE_AGENT); }
DI void st_agent(unsigned* p, unsigned v) { __hip_atomic_store((gu32*)p, v, __ATOMIC_RELAXED, __HIP_MEMORY_SCOPE_AGENT); }
DI float blo(unsigned u) { return __uint_as_float(u << 16); }
DI float bhi(unsigned u) { return __uint_as_float(u & 0xffff0000u); }
constexpr size_t XBUF_UNITS = (size_t)64 * 16 * 1024;
constexpr int SPIN_MAX = 1 << 18;

__global__ __launch_bounds__(768, 3) void fused_kernel(
    const __hip_bfloat16* __restrict__ xA, const __hip_bfloat16* __restrict__ kA, const __hip_bfloat16* __restrict__ vA,
    const __hip_bfloat16* __restrict__ wqA, const __hip_bfloat16* __restrict__ woA,
    const __hip_bfloat16* __restrict__ xB, const __hip_bfloat16* __restrict__ kB, const __hip_bfloat16* __restrict__ vB,
    const __hip_bfloat16* __restrict__ wqB, const __hip_bfloat16* __restrict__ woB, u32x4* __restrict__ part,
    __hip_bfloat16* __restrict__ out) {
  __shared__ __attribute__((aligned(1024))) char smem[LDS_BYTES];
  const unsigned L0 = (unsigned)(uintptr_t)(LAS char*)smem;
  const int tid = threadIdx.x;
  const int wave = __builtin_amdgcn_readfirstlane(tid >> 6), w = wave & 3;
  const int grp = blockIdx.x & 7, b = blockIdx.x >> 3, br = grp >> 2, hg = grp & 3, hbase = hg * HPG;
  const unsigned rq = b & 15, rs = b & 3;
  const unsigned rm = (hg + 1) & 3;
#define LANEVARS                                                                                  \
  int lane = tid & 63; OPAQUE(lane);                                                              \
  const int i16 = lane & 15, g = lane >> 4, qq = i16 >> 2, pp = i16 & 3;                          \
  const unsigned rho = 4 * (g & 1) + qq;

  if (wave < 4) {
    LANEVARS
    unsigned ka[4];
#pragma unroll
    for (int s = 0; s < 4; ++s) ka[s] = L0 + K_OFF + 256u * i16 + 16u * ((4 * s + g) ^ i16);
    unsigned tv = L0 + V_OFF + 256u * (4 * g + qq) + 32u * rho + 8u * pp;
    unsigned obw = L0 + OB_OFF + w * 1024 + lane * 16; OPAQUE(obw);
    bf16x8 ones = {0x3f80, 0x3f80, 0x3f80, 0x3f80, 0x3f80, 0x3f80, 0x3f80, 0x3f80};
    bf16x8 qf[4], fk[8], fv[8];
    f32x4 sa[2], sb[2], o[8], ol;
    f32x4 nm;
#define KF(c, SO) lds_b128(ka[(c) & 3] + (SO) + 4096u * ((c) >> 2))
#define KRELOAD(c, SO) asm volatile("ds_read_b128 %0, %1 offset:%2" : "+v"(fk[c]) : "v"(ka[(c) & 3]), "i"((SO) + 4096 * ((c) >> 2)) : "memory")
#define VF(c, SO) tr_frag<4096>((tv ^ (32u * (c))) + (SO))
#define QLOAD() _Pragma("unroll") for (int s = 0; s < 4; ++s) qf[s] = lds_b128(obw + s * 4096)
#define REFCHK(SC, FIRST)                                                                         \
    do {                                                                                          \
      float mx = fmax3(fmax3(SC[0][0], SC[0][1], SC[0][2]), fmax3(SC[0][3], SC[1][0], SC[1][1]), fmaxf(SC[1][2], SC[1][3])); \
      if (FIRST) {                                                                                \
        mx = xmax16_32(mx); nm = f32x4{-mx, -mx, -mx, -mx}; OPAQUE(nm); SC[0] -= mx; SC[1] -= mx;  \
      } else if (__builtin_expect(__any(mx > THR), 0)) {     \
        const float d = fmaxf(xmax16_32(mx), 0.f), alpha = __builtin_amdgcn_exp2f(-d);            \
        nm -= d; OPAQUE(nm); SC[0] -= d; SC[1] -= d;                                              \
        asm volatile("s_nop 7");                                                                  \
        _Pragma("unroll") for (int c = 0; c < 8; ++c) { OPAQUE(o[c]); o[c] *= alpha; }            \
        OPAQUE(ol); ol *= alpha;                                                                  \
      }                                                                                           \
    } while (0)
#define ASTEP(SC, SN, p)                                                                          \
    do {                                                                                          \
      const unsigned so = (((p) + 2) & 3) * 8192;                                                 \
      BAR();                                                                                      \
      OPAQUE(tv);                                                                                 \
      if ((p) == 15) { QLOAD(); SB(); }                                                           \
      u32x4 pw;                                                                                   \
      _Pragma("unroll") for (int c = 0; c < 8; ++c) {                                             \
        if ((c & 3) == 0 && (p) != 15) MF_C(SN[c >> 2], fk[c], qf[c & 3], nm); else MF((c & 3) == 0, SN[c >> 2], fk[c], qf[c & 3]); \
        KRELOAD(c, (((p) + 2) & 3) * 8192);                                                       \
        SC[c >> 2][c & 3] = __builtin_amdgcn_exp2f(SC[c >> 2][c & 3]);                            \
        if (c >= 2 && !(c & 1)) pw[(c >> 1) - 1] = pk2(SC[(c - 2) >> 2][(c - 2) & 3], SC[(c - 1) >> 2][(c - 1) & 3]); \
        SB();                                                                                     \
      }                                                                                           \
      pw[3] = pk2(SC[1][2], SC[1][3]);                                                            \
      bf16x8 pf = __builtin_bit_cast(bf16x8, pw);                                                 \
      NOPV2(pf, ones);                                                                            \
      MF((p) == 0, ol, ones, pf);                                                                 \
      _Pragma("unroll") for (int c = 0; c < 8; ++c) { MF((p) == 0, o[c], fv[c], pf); fv[c] = VF(c, so); SB(); } \
      asm volatile("" : "+v"(SN[0]), "+v"(SN[1]));                                                \
      if ((p) == 15) {                                                                            \
        asm volatile("s_nop 7");                                                                  \
        OPAQUE(ol);                                                                               \
        _Pragma("unroll") for (int c = 0; c < 8; ++c) OPAQUE(o[c]);                               \
        const float inv = 1.f / ol[0];                                                            \
        _Pragma("unroll") for (int s = 0; s < 4; ++s) *(LAS bf16x8*)(uintptr_t)(obw + s * 4096) = pack8(o[2 * s] * inv, o[2 * s + 1] * inv); \
      }                                                                                           \
      REFCHK(SN, (p) == 15);                                                                      \
    } while (0)
#pragma unroll
    for (int e = 0; e < 5; ++e) BAR();
#pragma unroll
    for (int c = 0; c < 8; ++c) fk[c] = KF(c, 0);
    BAR();
    QLOAD();
#pragma unroll
    for (int c = 0; c < 8; ++c) fv[c] = VF(c, 8192);
    SB();
#pragma unroll
    for (int c = 0; c < 8; ++c) { MF((c & 3) == 0, sa[c >> 2], fk[c], qf[c & 3]); fk[c] = KF(c, 8192); SB(); }
    NOPD2(sa[0], sa[1]);
    REFCHK(sa, true);
#pragma nounroll
    for (int h = 0; h < HPG; ++h) {
#pragma unroll
      for (int p2 = 0; p2 < 16; p2 += 2) { ASTEP(sa, sb, p2); ASTEP(sb, sa, p2 + 1); }
    }
    BAR(); BAR();
  } else if (wave < 8) {
    LANEVARS
    unsigned* hs = (unsigned*)(part + XBUF_UNITS) + (size_t)(blockIdx.x & ~3) * 128;
    const int pj = lane & 3;
    const bool hsl = w == 0 && lane < 4 && pj != hg;
    unsigned nonce = 0, pn = 0;
    unsigned e0 = 0, d0 = 0;
    if (hsl) { e0 = ld_agent(hs + pj * 128 + 32 + hg); d0 = ld_agent(hs + pj * 128 + 64 + hg); }
    bf16x8 xf[16];
    {
      const char* xr = (const char*)((br ? xB : xA) + ((size_t)b * SQ + 16 * w + i16) * DM) + 16 * g;
#pragma unroll
      for (int s = 0; s < 16; ++s) xf[s] = *(const bf16x8*)(xr + 64 * ((s + rq) & 15));
    }
    unsigned tq[8];
#pragma unroll
    for (int c = 0; c < 8; ++c) { tq[c] = (L0 + WQ_OFF + 256u * (8 * g + qq) + 32u * rho + 8u * pp) ^ (32u * c); OPAQUE(tq[c]); }
    unsigned qw = L0 + OB_OFF + w * 1024 + (i16 + 16 * (g >> 1)) * 16 + 8 * (g & 1); OPAQUE(qw);
    f32x4 qacc[8];
    bf16x8 fa[8];
#define WQF(c, TOFF) tr_frag<1024>(tq[c] + (TOFF))
#define QFIN()                                                                                    \
    NOPD2(qacc[6], qacc[7]);                                                                      \
    _Pragma("unroll") for (int c = 0; c < 8; ++c) {                                               \
      OPAQUE(qacc[c]);                                                                            \
      const f32x4 a = qacc[c] * QSCALE;                                                           \
      u32x2 v = {pk2(a[0], a[1]), pk2(a[2], a[3])};                                               \
      *(LAS u32x2*)(uintptr_t)(qw + (c >> 1) * 4096 + (c & 1) * 512) = v;                         \
    }
#pragma unroll
    for (int s = 0; s < 16; ++s) {
      if ((s & 3) == 0) BAR();
#pragma unroll
      for (int c = 0; c < 8; ++c) {
        if (s > 0) MF(s == 1, qacc[c], fa[c], xf[s - 1]);
        fa[c] = WQF(c, ((s & 3) - 2) * 32768 + (s >> 2) * 8192);
        SB();
      }
    }
    BAR();
#pragma unroll
    for (int c = 0; c < 8; ++c) { MF_ACC(qacc[c], fa[c], xf[15]); fa[c] = WQF(c, 0); SB(); }
    QFIN();
    if (w == 0) {
      const unsigned long long be = __ballot((~e0 >> (2 * pj)) & 1u), bd = __ballot((~d0 >> (2 * pj + 1)) & 1u);
#pragma unroll
      for (int j = 0; j < 4; ++j) nonce |= (unsigned)((be >> j) & 1ull) << (2 * j) | (unsigned)((bd >> j) & 1ull) << (2 * j + 1);
      if (lane == 0) { st_agent(hs + hg * 128, nonce); *(LAS unsigned*)(uintptr_t)(L0 + HS_OFF) = nonce; }
    }
#pragma nounroll
    for (int h = 0; h < HPG - 1; ++h) {
      if (h == 1 && hsl) pn = ld_agent(hs + pj * 128);
      if (h == 2) { VMWAIT(); if (hsl) st_agent(hs + hg * 128 + 32 + pj, pn); }
#pragma unroll
      for (int j = 0; j < 16; ++j) {
        BAR();
#pragma unroll
        for (int c = 0; c < 8; ++c) {
          MF(j == 0, qacc[c], fa[c], xf[j]);
          fa[c] = WQF(c, ((j + 1) & 3) * 8192);
          SB();
        }
        if (j == 15) { QFIN(); }
      }
    }
    bool known = !hsl;
    {
      unsigned e = 0;
      if (hsl) e = ld_agent(hs + pj * 128 + 32 + hg);
      BAR(); BAR(); BAR(); BAR();
      VMWAIT();
      if (hsl) pn = ld_agent(hs + pj * 128);
      BAR(); BAR(); BAR(); BAR();
      if (hsl) { st_agent(hs + hg * 128 + 32 + pj, pn); known = e == nonce; }
    }
#pragma nounroll
    for (int P = 125; P < 136; ++P) BAR();
    if (w == 0) {
      for (int sp = 0; sp < SPIN_MAX && !__all(known); ++sp) {
        if (!known) {
          const unsigned e = ld_agent(hs + pj * 128 + 32 + hg);
          VMWAIT();
          pn = ld_agent(hs + pj * 128);
          st_agent(hs + hg * 128 + 32 + pj, pn);
          known = e == nonce;
        }
        __builtin_amdgcn_s_sleep(4);
      }
      if (hsl) st_agent(hs + hg * 128 + 64 + pj, pn);
    }
  } else {
    LANEVARS
    __builtin_amdgcn_s_setprio(2);
    const void* pwq = br ? wqB : wqA;
    const void* pwo = br ? woB : woA;
    const unsigned ring = L0 + 32768u * w;
    Stream st;
    stream_set(st, 2, pwq, DM * NH * DH * 2, lane, 0, 4, 2, w + rq, 15, 18, 0, 0, 0, 0, 0, hbase * 256u, false);
    unsigned vb = 0;
    stream_issue(st, 0, ring, vb); stream_issue(st, 1, ring, vb); stream_issue(st, 2, ring, vb);
    f32x4 acc[4][2][4];
#pragma unroll
    for (int m = 0; m < 4; ++m)
#pragma unroll
      for (int e = 0; e < 2; ++e)
#pragma unroll
        for (int q = 0; q < 4; ++q) acc[m][e][q] = Z4;
    bf16x8 of[4];
    const unsigned tw = L0 + WQ_OFF + 256u * (4 * g + qq) + 32u * rho + 8u * pp;
    unsigned tw0 = tw ^ (32u * (2 * w)), tw1 = tw ^ (32u * (2 * w + 1)), ob = L0 + OB_OFF + lane * 16;
    asm volatile("" : "+v"(tw0), "+v"(tw1), "+v"(ob));
#define OLOAD(S) _Pragma("unroll") for (int q = 0; q < 4; ++q) of[q] = lds_b128(ob + ((((S) + rs) & 3) << 12) + q * 1024)
#define WOF(T, TOFF) tr_frag<4096>((T) + (TOFF))
    WAITBAR(16);
    stream_issue(st, 3, ring, vb);
    {
      const size_t kvoff = ((size_t)b * NH + hbase) * SK * DH;
      if (w < 2) stream_set(st, w, (w ? (br ? vB : vA) : (br ? kB : kA)) + kvoff, HPG * SK * DH * 2, lane, 4 + w, 128, 0, 0, 0, 0, 0, 0, 0xffffu, 13, 0, 0, true);
      else if (w == 2) stream_set(st, 2, pwq, DM * NH * DH * 2, lane, 4, 112, 0, rq, 15, 18, 0, 0, 0, 0, 256u, (hbase + 1) * 256u, false);
      else stream_set(st, 3, pwo, DM * NH * DH * 2, lane, 21, 112, 0, rm, 3, 8, 2, rs, 3, 15, 131072u, hbase * 131072u, false);
    }
#pragma nounroll
    for (int P = 1; P < 22; ++P) { SWAITBAR(); stream_issue(st, P + 3, ring, vb); }
    bf16x8 f0 = WOF(tw0, (WO_OFF - WQ_OFF) + 8192), f1 = WOF(tw1, (WO_OFF - WQ_OFF) + 8192);
#pragma nounroll
    for (int it = 0; it < 28; ++it) {
      if (it == 27)
        stream_set(st, 3, pwo, DM * NH * DH * 2, fresh_lane(), 133, 4, 0, rm, 3, 8, 0, 0, 0, 0, 0, (hbase + 7) * 131072u + w * 32768u, false);
#pragma unroll
      for (int m = 0; m < 4; ++m) {
        unsigned so, lds;
        unsigned pol = stream_addr(st, 25 + 4 * it + m, ring, so, lds);
        asm volatile("" : "+s"(so), "+s"(lds), "+s"(pol));
        SWAITBAR();
        vb = 2 * vb + (pol != 0);
        if (m == 0) { OLOAD(it & 3); }
        stream_half(st, so, lds, pol);
#pragma unroll
        for (int q = 0; q < 4; ++q) MF_ACC(acc[m][0][q], f0, of[q]);
        f0 = WOF(tw0, (WO_OFF - WQ_OFF) + ((m + 2) & 3) * 8192);
        SB();
#pragma unroll
        for (int q = 0; q < 4; ++q) MF_ACC(acc[m][1][q], f1, of[q]);
        f1 = WOF(tw1, (WO_OFF - WQ_OFF) + ((m + 2) & 3) * 8192);
        SB();
        stream_half(st, so, lds, pol);
      }
    }
    WAITBAR(0);
    const int ln = fresh_lane();
    const __amdgpu_buffer_rsrc_t sx = __builtin_amdgcn_make_buffer_rsrc((void*)(part + (size_t)(blockIdx.x >> 2) * 16384), (short)0, 16384 * 16, 0x00020000);
    unsigned te0 = tw0 - 65536u, te1 = tw1 - 65536u;
    asm volatile("" : "+v"(te0), "+v"(te1));
#define EWOF(X, i) WOF((((i) & 3) < 2 ? te##X : tw##X), ((i) & 1) * 32768 + ((((i) >> 2) + 1) & 3) * 8192)
#pragma unroll
    for (int q = 0; q < 4; ++q) of[q] = lds_b128(ob + q * 1024);
    f0 = EWOF(0, 0); f1 = EWOF(1, 0);
#pragma unroll
    for (int i = 0; i < 16; ++i) {
      const int m = i >> 2;
#pragma unroll
      for (int q = 0; q < 4; ++q) MF_ACC(acc[m][0][q], f0, of[q]);
      if (i < 15) f0 = EWOF(0, i + 1);
      SB();
#pragma unroll
      for (int q = 0; q < 4; ++q) {
        MF_ACC(acc[m][1][q], f1, of[q]);
        if (i < 15) of[q] = lds_b128(ob + ((i + 1) & 3) * 4096 + q * 1024);
        SB();
      }
      if (i < 15) f1 = EWOF(1, i + 1);
      SB();
      if ((i & 3) == 3) {
        asm volatile("s_nop 7");
#pragma unroll
        for (int q = 0; q < 4; ++q) { OPAQUE(acc[m][0][q]); OPAQUE(acc[m][1][q]); }
        if (m < 3) {
          const unsigned so = ((((m + rm) & 3) * 4 + hg) * 1024 + w * 256) * 16;
#pragma unroll
          for (int q = 0; q < 4; ++q)
            __builtin_amdgcn_raw_buffer_store_b128(__builtin_bit_cast(u32x4, pack8(acc[m][0][q], acc[m][1][q])), sx, ln * 16 + q * 1024, so, 16);
          asm volatile("s_nop 1" ::: "memory");
          SB();
        }
      }
    }
    WAITBAR(0);
    {
      const unsigned nonce = *(const LAS unsigned*)(uintptr_t)(L0 + HS_OFF);
      const unsigned* hs = (const unsigned*)(part + XBUF_UNITS) + (size_t)(blockIdx.x & ~3) * 128;
      const int pj = ln & 3;
      for (int sp = 0; sp < SPIN_MAX; ++sp) {
        const unsigned d = ld_agent(hs + pj * 128 + 64 + hg);
        if (__all(pj == hg || d == nonce)) break;
        __builtin_amdgcn_s_sleep(1);
      }
      __builtin_amdgcn_fence(__ATOMIC_ACQUIRE, "wavefront");
      u32x4 r[3][4];
#pragma unroll
      for (int j = 0; j < 3; ++j)
#pragma unroll
        for (int q = 0; q < 4; ++q)
          r[j][q] = __builtin_amdgcn_raw_buffer_load_b128(sx, ln * 16 + q * 1024, ((hg * 4 + ((hg + 1 + j) & 3)) * 1024 + w * 256) * 16, 16);
      const int g2 = ln >> 4;
      __hip_bfloat16* op = out + ((size_t)b * SQ + (ln & 15)) * (2 * DM) + br * DM + hg * 128 + w * 32 + (g2 & 1) * 16 + (g2 >> 1) * 8;
#pragma unroll
      for (int q = 0; q < 4; ++q) {
        unsigned v[4];
#pragma unroll
        for (int i = 0; i < 4; ++i) {
          const f32x4& a = acc[3][i >> 1][q];
          v[i] = pk2((a[(i & 1) * 2] + blo(r[0][q][i])) + (blo(r[1][q][i]) + blo(r[2][q][i])),
                     (a[(i & 1) * 2 + 1] + bhi(r[0][q][i])) + (bhi(r[1][q][i]) + bhi(r[2][q][i])));
        }
        auto s0 = __builtin_amdgcn_permlane16_swap(v[0], v[2], false, false);
        auto s1 = __builtin_amdgcn_permlane16_swap(v[1], v[3], false, false);
        *(u32x4*)(op + (size_t)q * 16 * (2 * DM)) = u32x4{s0[0], s1[0], s0[1], s1[1]};
      }
    }
  }
}

extern "C" void kernel_launch(void* const* d_in, const int* in_sizes, int n_in, void* d_out, int out_size, void* d_ws,
                              size_t ws_size, hipStream_t stream) {
  const __hip_bfloat16* const* in = (const __hip_bfloat16* const*)d_in;
  u32x4* part = (u32x4*)d_ws;
  fused_kernel<<<256, 768, 0, stream>>>(in[0], in[1], in[2], in[3], in[4], in[5], in[6], in[7], in[8], in[9], part, (__hip_bfloat16*)d_out);
}
```
